```python
import torch

def _softmax(x, axis=-1):
    return torch.softmax(x, dim=axis)
def _gelu(x):
    return torch.nn.functional.gelu(x, approximate="tanh")
def _silu(x):
    return torch.nn.functional.silu(x)
def _sigmoid(x):
    return torch.sigmoid(x)
def _layernorm(x, axis=-1, eps=1e-5):
    m = x.mean(dim=axis, keepdim=True)
    v = x.var(dim=axis, keepdim=True, unbiased=False)
    return (x - m) / torch.sqrt(v + eps)
def _scatter_add(tgt, idx, src, axis=0):
    return tgt.clone().index_add_(axis, idx, src)
def _segment_sum(src, seg, n):
    out = torch.zeros((n,) + src.shape[1:], dtype=src.dtype)
    return out.index_add_(0, seg, src)
def _pad(x, pw):
    flat = tuple(v for pair in reversed(pw) for v in pair)
    return torch.nn.functional.pad(x, flat)

kernel_name = 'gated_mix__sliding_block_cached__dense_activation_mish__decode'

def setup_inputs():

    _w = (lambda a: (lambda t: t.to(torch.bfloat16) if t.is_floating_point() else t)(torch.as_tensor(a)))
    def _A_setup():
        B, Sq, D = 1, 1, 512
        Hq, Hkv, Sk, Dh = 32, 8, 65536, 128
        assert Hq % Hkv == 0, (Hq, Hkv)
        return {
            "x": _w((lambda *s: torch.randn(*s, dtype=torch.bfloat16) * 0.05)(B, Sq, D)),
            "k_cache": _w((lambda *s: torch.randn(*s, dtype=torch.bfloat16) * 6.4)(B, Hkv, Sk, Dh)),
            "v_cache": _w((lambda *s: torch.randn(*s, dtype=torch.bfloat16) * 0.4)(B, Hkv, Sk, Dh)),
            "wq": _w((lambda *s: torch.randn(*s, dtype=torch.bfloat16) * 6.4)(D, Hq * Dh) * (D ** -0.5)),
            "wo": _w((lambda *s: torch.randn(*s, dtype=torch.bfloat16) * 0.4)(Hq * Dh, D) * ((Hq * Dh) ** -0.5)),
        }
    _A_in = _A_setup()

    def _B_setup():
        B, S, D, F = 1, 1, 512, 1792
        return {
            "x": _w((lambda *s: torch.randn(*s, dtype=torch.bfloat16) * 0.05)(B, S, D)),
            "w1": _w((lambda *s: torch.randn(*s, dtype=torch.bfloat16) * 12.8)(D, F) * (D ** -0.5)),
            "b1": _w((lambda *s: torch.randn(*s, dtype=torch.bfloat16) * 0.05)(F)),
            "w2": _w((lambda *s: torch.randn(*s, dtype=torch.bfloat16) * 0.05)(F, D) * (F ** -0.5)),
        }
    _B_in = _B_setup()

    return {
        "A_x": _A_in["x"],
        "A_k_cache": _A_in["k_cache"],
        "A_v_cache": _A_in["v_cache"],
        "A_wq": _A_in["wq"],
        "A_wo": _A_in["wo"],
        "B_x": _B_in["x"],
        "B_w1": _B_in["w1"],
        "B_b1": _B_in["b1"],
        "B_w2": _B_in["w2"],
        "gate_w": _w((lambda *s: torch.randn(*s, dtype=torch.bfloat16) * 0.05)(512) * 16.0),
    }

def reference(A_x, A_k_cache, A_v_cache, A_wq, A_wo, B_x, B_w1, B_b1, B_w2, gate_w):
    def _A_ref(x, k_cache, v_cache, wq, wo):
        B, Sq, D = x.shape
        Hq, Dh = 32, 128
        Hkv, Sk = k_cache.shape[1], k_cache.shape[2]
        W = min(4096, max(256, Sk // 4))
        rep = Hq // Hkv
        q = (lambda x, *d: x.permute(*d))((x @ wq).reshape(B, Sq, Hq, Dh), 0, 2, 1, 3)
        k = (lambda x, n, axis: torch.repeat_interleave(x, n, dim=axis))(k_cache[:, :, Sk - W:, :], rep, axis=1)
        v = (lambda x, n, axis: torch.repeat_interleave(x, n, dim=axis))(v_cache[:, :, Sk - W:, :], rep, axis=1)
        scores = torch.einsum("bhqd,bhkd->bhqk", q, k) * (Dh ** -0.5)
        p = _softmax(scores, axis=-1)
        o = torch.einsum("bhqk,bhkd->bhqd", p, v)
        return (lambda x, *d: x.permute(*d))(o, 0, 2, 1, 3).reshape(B, Sq, Hq * Dh) @ wo

    def _B_ref(x, w1, b1, w2):
        h = torch.einsum("bsd,df->bsf", x, w1) + b1
        sp = torch.log1p(torch.exp(-torch.abs(h))) + torch.where(h > 0, h, torch.zeros_like(h))
        act = h * torch.tanh(sp)
        return torch.einsum("bsf,fd->bsd", act, w2)

    x = A_x
    g = _sigmoid((x * gate_w).sum(-1, keepdims=True))
    a = _A_ref(x, A_k_cache, A_v_cache, A_wq, A_wo)
    b = _B_ref(x, B_w1, B_b1, B_w2)
    return g * a + (1 - g) * b
```

```cpp
#include <hip/hip_runtime.h>
#include <hip/hip_bf16.h>
#include <stdint.h>

typedef unsigned short u16;
__device__ __forceinline__ float bf2f(u16 v) { return __uint_as_float(((unsigned)v) << 16); }
__device__ __forceinline__ u16 f2bf(float f) { unsigned u = __float_as_uint(f); u += 0x7FFF + ((u >> 16) & 1); return (u16)(u >> 16); }

constexpr int D = 512, HQ = 32, HKV = 8, SK = 65536, DH = 128, W = 4096, F = 1792;
constexpr int NSPLIT = 32, KPS = W / NSPLIT;

__global__ void __launch_bounds__(256) k1(const u16* x, const u16* wq, const u16* w1, const u16* b1, float* q, float* act) {
  __shared__ float xs[D];
  __shared__ float red[4][64];
  int tid = threadIdx.x;
  for (int i = tid; i < D; i += 256) xs[i] = bf2f(x[i]);
  __syncthreads();
  int col = blockIdx.x * 64 + (tid & 63);
  int part = tid >> 6;
  float acc = 0.f;
  bool isq = col < HQ * DH;
  const u16* w = isq ? wq + col : w1 + (col - HQ * DH);
  int ld = isq ? HQ * DH : F;
  for (int r = part * 128; r < part * 128 + 128; ++r) acc += xs[r] * bf2f(w[(size_t)r * ld]);
  red[part][tid & 63] = acc;
  __syncthreads();
  if (tid < 64) {
    float s = red[0][tid] + red[1][tid] + red[2][tid] + red[3][tid];
    if (isq) q[col] = bf2f(f2bf(s));
    else {
      int c = col - HQ * DH;
      float h = s + bf2f(b1[c]);
      float sp = log1pf(expf(-fabsf(h))) + (h > 0.f ? h : 0.f);
      act[c] = h * tanhf(sp);
    }
  }
}

__global__ void __launch_bounds__(256) k2(const float* q, const u16* kc, const u16* vc, float* pm, float* pl, float* po) {
  __shared__ float qs[4][DH];
  __shared__ float sc[4][KPS];
  int h = blockIdx.x & 7, s = blockIdx.x >> 3, tid = threadIdx.x;
  for (int i = tid; i < 4 * DH; i += 256) qs[i / DH][i % DH] = q[h * 4 * DH + i];
  __syncthreads();
  const u16* kb = kc + ((size_t)h * SK + (SK - W) + s * KPS) * DH;
  const u16* vb = vc + ((size_t)h * SK + (SK - W) + s * KPS) * DH;
  for (int i = tid; i < 4 * KPS; i += 256) {
    int g = i / KPS, k = i % KPS;
    float a = 0.f;
    for (int d = 0; d < DH; ++d) a += qs[g][d] * bf2f(kb[k * DH + d]);
    sc[g][k] = a * 0.08838834764831845f;
  }
  __syncthreads();
  __shared__ float ms[4], ls[4];
  if (tid < 4) {
    float m = -INFINITY;
    for (int k = 0; k < KPS; ++k) m = fmaxf(m, sc[tid][k]);
    float l = 0.f;
    for (int k = 0; k < KPS; ++k) { float p = expf(sc[tid][k] - m); sc[tid][k] = p; l += p; }
    ms[tid] = m; ls[tid] = l;
    pm[(h * 4 + tid) * NSPLIT + s] = m; pl[(h * 4 + tid) * NSPLIT + s] = l;
  }
  __syncthreads();
  for (int i = tid; i < 4 * DH; i += 256) {
    int g = i / DH, d = i % DH;
    float a = 0.f;
    for (int k = 0; k < KPS; ++k) a += sc[g][k] * bf2f(vb[k * DH + d]);
    po[((size_t)(h * 4 + g) * NSPLIT + s) * DH + d] = a;
  }
}

__global__ void __launch_bounds__(128) k3(const float* pm, const float* pl, const float* po, float* o) {
  int hq = blockIdx.x, d = threadIdx.x;
  float M = -INFINITY;
  for (int s = 0; s < NSPLIT; ++s) M = fmaxf(M, pm[hq * NSPLIT + s]);
  float L = 0.f, a = 0.f;
  for (int s = 0; s < NSPLIT; ++s) { float w = expf(pm[hq * NSPLIT + s] - M); L += w * pl[hq * NSPLIT + s]; a += w * po[((size_t)hq * NSPLIT + s) * DH + d]; }
  o[hq * DH + d] = a / L;
}

__global__ void __launch_bounds__(256) k4(const u16* x, const u16* gw, const float* o, const float* act, const u16* wo, const u16* w2, u16* out) {
  __shared__ float red[2][32][8];
  int tid = threadIdx.x;
  int col = blockIdx.x * 8 + (tid & 7), part = tid >> 3;
  float a = 0.f, b = 0.f;
  for (int r = part; r < HQ * DH; r += 32) a += o[r] * bf2f(wo[(size_t)r * D + col]);
  for (int r = part; r < F; r += 32) b += act[r] * bf2f(w2[(size_t)r * D + col]);
  red[0][part][tid & 7] = a; red[1][part][tid & 7] = b;
  __syncthreads();
  if (tid < 8) {
    float A = 0.f, B = 0.f;
    for (int p = 0; p < 32; ++p) { A += red[0][p][tid]; B += red[1][p][tid]; }
    float z = 0.f;
    for (int i = 0; i < D; ++i) z += bf2f(x[i]) * bf2f(gw[i]);
    float g = 1.f / (1.f + expf(-z));
    out[col] = f2bf(g * A + (1.f - g) * B);
  }
}

extern "C" void kernel_launch(void* const* d_in, const int* in_sizes, int n_in,
                              void* d_out, int out_size, void* d_ws, size_t ws_size,
                              hipStream_t stream) {
  const u16* x = (const u16*)d_in[0];
  const u16* kc = (const u16*)d_in[1];
  const u16* vc = (const u16*)d_in[2];
  const u16* wq = (const u16*)d_in[3];
  const u16* wo = (const u16*)d_in[4];
  const u16* w1 = (const u16*)d_in[6];
  const u16* b1 = (const u16*)d_in[7];
  const u16* w2 = (const u16*)d_in[8];
  const u16* gw = (const u16*)d_in[9];
  float* ws = (float*)d_ws;
  float* q = ws; float* act = ws + 4096; float* pm = ws + 8192; float* pl = pm + 1024; float* o = pl + 1024; float* po = o + 4096;
  k1<<<(4096 + 1792) / 64, 256, 0, stream>>>(x, wq, w1, b1, q, act);
  k2<<<256, 256, 0, stream>>>(q, kc, vc, pm, pl, po);
  k3<<<32, 128, 0, stream>>>(pm, pl, po, o);
  k4<<<64, 256, 0, stream>>>(x, gw, o, act, wo, w2, (u16*)d_out);
}
```

```cpp
#include <hip/hip_runtime.h>
#include <stdint.h>

typedef unsigned u32;
typedef unsigned long long u64;
typedef __attribute__((ext_vector_type(2))) unsigned u32x2;
typedef __attribute__((ext_vector_type(4))) unsigned u32x4;
typedef __attribute__((ext_vector_type(4))) float f32x4;
typedef __attribute__((ext_vector_type(4))) short s16x4;
typedef __attribute__((ext_vector_type(8))) __bf16 bf16x8;
typedef __attribute__((ext_vector_type(2))) __bf16 bf16x2;
#define LAS __attribute__((address_space(3)))
#define CAS __attribute__((address_space(4)))

constexpr u32 L_WQ = 0, L_W1 = 16384, L_K = 24576, L_V = 57344, L_W2 = 90112, L_WO = 98304;
constexpr u32 L_X = 114688, L_GW = 115712, L_JUNK = 116736, L_Q = 117760, L_P = 119040, L_M = 120064, L_O = 120128, L_A = 120384, L_END = 120512;
constexpr u32 W_ACC = 4096;
constexpr u32 W_QP = 16384;
constexpr u32 W_HP = W_QP + 65536;
constexpr u32 W_ML = W_HP + 73728;
constexpr u32 W_FP = W_ML + 16384;
constexpr u32 W_OP = W_FP + 131072;

#define STAMP(i)
#define SLEEP1 0
#define SLEEP2 0
#define KWAIT 8

#define DMA(ldsaddr, voff, base, pol) asm volatile("s_mov_b32 m0, %0\n\ts_nop 0\n\tglobal_load_lds_dwordx4 %1, %2 " pol :: "s"(ldsaddr), "v"(voff), "s"(base) : "memory")
#define WAITV(n) asm volatile("s_waitcnt vmcnt(" #n ")" ::: "memory")
#define WAITV_(n) WAITV(n)
#define BARV(n) asm volatile("s_waitcnt vmcnt(" #n ") lgkmcnt(0)\n\ts_barrier" ::: "memory")
#define BAR() asm volatile("s_waitcnt lgkmcnt(0)\n\ts_barrier" ::: "memory")

__device__ __forceinline__ f32x4 mfma(u32x4 a, u32x4 b, f32x4 c) {
  return __builtin_amdgcn_mfma_f32_16x16x32_bf16(__builtin_bit_cast(bf16x8, a), __builtin_bit_cast(bf16x8, b), c, 0, 0, 0);
}
__device__ __forceinline__ u32x4 ldsr128(u32 a) { return *(const LAS u32x4*)(size_t)a; }
__device__ __forceinline__ u32x2 ldstr(u32 a) { return __builtin_bit_cast(u32x2, __builtin_amdgcn_ds_read_tr16_b64_v4i16((LAS s16x4*)(size_t)a)); }
__device__ __forceinline__ u32x4 ldstr2(u32 a, u32 b) { u32x2 x = ldstr(a), y = ldstr(b); return u32x4{x[0], x[1], y[0], y[1]}; }
__device__ __forceinline__ u32 pkbf(float lo, float hi) { bf16x2 v = {(__bf16)lo, (__bf16)hi}; return __builtin_bit_cast(u32, v); }
__device__ __forceinline__ u32 f2u(float x) { return __float_as_uint(x); }
__device__ __forceinline__ float u2f(u32 x) { return __uint_as_float(x); }
__device__ __forceinline__ u32 tagf(float v, u32 t) { return (f2u(v) & 0xFFFFFF00u) | t; }
template <int CTRL> __device__ __forceinline__ float dpp(float x) { return u2f(__builtin_amdgcn_mov_dpp(f2u(x), CTRL, 0xf, 0xf, true)); }
constexpr int XOR1 = 0xB1, XOR2 = 0x4E, ROR4 = 0x124, ROR8 = 0x128;
__device__ __forceinline__ float sw32_add(float a, float b) { auto r = __builtin_amdgcn_permlane32_swap(f2u(a), f2u(b), false, false); return u2f(r[0]) + u2f(r[1]); }
__device__ __forceinline__ float sw16_add(float a, float b) { auto r = __builtin_amdgcn_permlane16_swap(f2u(a), f2u(b), false, false); return u2f(r[0]) + u2f(r[1]); }
__device__ __forceinline__ float sw32_max(float a) { auto r = __builtin_amdgcn_permlane32_swap(f2u(a), f2u(a), false, false); return fmaxf(u2f(r[0]), u2f(r[1])); }
__device__ __forceinline__ float sw16_max(float a) { auto r = __builtin_amdgcn_permlane16_swap(f2u(a), f2u(a), false, false); return fmaxf(u2f(r[0]), u2f(r[1])); }
__device__ __forceinline__ float dot2(u32 a, u32 b, float c) { return __builtin_amdgcn_fdot2_f32_bf16(__builtin_bit_cast(bf16x2, a), __builtin_bit_cast(bf16x2, b), c, false); }
constexpr u32 SPIN_CAP = 1u << 20;

__global__ void __launch_bounds__(256, 1)
gm_kernel(const char* wq, const char* w1, const char* kc, const char* x, char* ws, const char* gw, const char* vc,
          const char* wo, const char* w2, const char* b1, char* out) {
  __shared__ __attribute__((aligned(1024))) char lds_[L_END];
  asm volatile("" :: "v"((LAS char*)lds_) : "memory");
  STAMP(0);
  const u32 b = blockIdx.x, h = b & 7, s = b >> 3, cg = s & 7, ks = s >> 3, hq = h * 4 + ks;
  const u32 t = threadIdx.x, l = t & 63, w = __builtin_amdgcn_readfirstlane(t >> 6);
  const u32 n = l & 15, g = l >> 4;
  const u32 accoff = W_ACC + (cg * 4 + w) * 256 + n * 8;

  if (h == 0 && ks == 0) {
    if (l < 16) { u64 z = 0; asm volatile("global_store_dwordx2 %0, %1, %2 sc1" :: "v"(accoff), "v"(z), "s"(ws) : "memory"); }
  }
  const u32 grow = 8 * w + (l >> 3);
  const u32 gchk = ((l & 7) ^ ((((l >> 4) & 1) | ((w & 1) << 1)) << 1)) * 16;
  {
    const char* p = wq + (size_t)ks * (128 * 8192) + h * 1024 + cg * 128;
    const u32 vo = grow * 8192 + gchk;
#pragma unroll
    for (int i = 0; i < 4; ++i) DMA(L_WQ + w * 1024 + i * 4096, vo + i * 32 * 8192, p, "nt");
  }
  {
    u64 pc; asm volatile("s_getpc_b64 %0" : "=s"(pc));
    const char* p = w == 0 ? x : w == 1 ? gw : (const char*)(pc & ~127ull) + (w - 2) * 3584;
    const u32 vo = w < 2 ? l * 16 : (l < 28 ? l : 0) * 128;
    DMA(L_X + (w < 2 ? w : 2) * 1024, vo, p, "");
  }
  const u32 cg1 = b % 28, ks8 = b / 28;
  {
    const bool on = b < 224;
    const char* p = w1 + (on ? ks8 * (64 * 3584) + cg1 * 128 : 0);
    const u32 vo = on ? grow * 3584 + gchk : 0, st = on ? 32 * 3584 : 0;
#pragma unroll
    for (int i = 0; i < 2; ++i) DMA(L_W1 + w * 1024 + i * 4096, vo + i * st, p, "nt");
  }
  const size_t kvoff = ((size_t)h * 65536 + 61440) * 256 + s * 4096;
  const u32 krow = 4 * w + g;
  {
    const char* p = kc + kvoff;
    const u32 vo = krow * 256 + (n ^ krow) * 16;
#pragma unroll
    for (int i = 0; i < 8; ++i) DMA(L_K + w * 1024 + i * 4096, vo + i * 131072, p, "nt");
  }
  const u32 E = *(const CAS u32*)ws + 1;
  const u32 T0 = E & 255, T1 = (E + 67) & 255, T2 = (E + 134) & 255, T3 = (E + 201) & 255;
  const u32 Tn = (E + 67 * (l & 3)) & 255;
  asm volatile("" :: "s"(E));
  STAMP(1);

  const u32 q4 = (l >> 2) & 3, p4 = l & 3;
  const u32 gtr = (8 * g + q4) * 128 + (w ^ ((q4 >> 1) | ((g & 1) << 1))) * 32 + p4 * 8;
  const f32x4 zero4 = {0.f, 0.f, 0.f, 0.f};

  BARV(10);
  STAMP(2);
  {
    f32x4 acc = zero4;
    const u32 xa = L_X + ks * 256 + g * 16;
#pragma unroll
    for (int kk = 0; kk < 4; ++kk) acc = mfma(ldsr128(xa + kk * 64), ldstr2(gtr + L_WQ + kk * 4096, gtr + L_WQ + kk * 4096 + 512), acc);
    const u32 v = tagf(acc[0], Tn), o = W_QP + ((h * 4 + ks) * 512 + cg * 64 + w * 16 + n) * 4;
    if (l < 16) asm volatile("global_store_dword %0, %1, %2 sc1" :: "v"(o), "v"(v), "s"(ws) : "memory");
  }
  STAMP(3);
  BARV(9);
  {
    f32x4 acc = zero4;
    const u32 xa = L_X + (ks8 & 7) * 128 + g * 16;
#pragma unroll
    for (int kk = 0; kk < 2; ++kk) acc = mfma(ldsr128(xa + kk * 64), ldstr2(gtr + L_W1 + kk * 4096, gtr + L_W1 + kk * 4096 + 512), acc);
    const u32 v = tagf(acc[0], Tn), o = W_HP + (ks8 * 1792 + cg1 * 64 + w * 16 + n) * 4;
    if (l < 16) asm volatile("global_store_dword %0, %1, %2 sc1" :: "v"(o), "v"(v), "s"(ws) : "memory");
  }
  float gate;
  {
    const u32x4 a = ldsr128(L_X + l * 16), c = ldsr128(L_GW + l * 16);
    float z = dot2(a[0], c[0], 0.f); z = dot2(a[1], c[1], z); z = dot2(a[2], c[2], z); z = dot2(a[3], c[3], z);
    z += dpp<XOR1>(z); z += dpp<XOR2>(z); z += dpp<ROR4>(z); z += dpp<ROR8>(z);
    z = sw16_add(z, z); z = sw32_add(z, z);
    gate = __builtin_amdgcn_rcpf(1.f + __builtin_amdgcn_exp2f(-1.4426950408889634f * z));
    asm volatile("" : "+v"(gate));
  }
  STAMP(4);
  WAITV_(KWAIT);
  if (SLEEP1) __builtin_amdgcn_s_sleep(SLEEP1);
  STAMP(5);
  u32x4 qa, qb;
  const u32 qoff = W_QP + ((h * 4 + 2 * (l >> 5)) * 512 + w * 128 + (l & 31) * 4) * 4;
#define G1() asm volatile("global_load_dwordx4 %0, %2, %3 sc1\n\tglobal_load_dwordx4 %1, %2, %3 offset:2048 sc1" : "=&v"(qa), "=&v"(qb) : "v"(qoff), "s"(ws) : "memory")
  G1();
  {
    const char* p = vc + kvoff;
    const u32 vo = krow * 256 + (n ^ ((krow & 7) << 1)) * 16;
#pragma unroll
    for (int i = 0; i < 8; ++i) DMA(L_V + w * 1024 + i * 4096, vo + i * 131072, p, "nt");
  }
  u32x2 hp; u32 zf, b1v;
  const u32 hcol = (w * 16 + (l & 7) * 2 < 56) ? w * 16 + (l & 7) * 2 : 0;
  const u32 hoff = W_HP + ((l >> 3) * 1792 + hq * 56 + hcol) * 4;
  const u32 zoff = W_QP + (cg * 64 + n) * 4;
#define GH() asm volatile("global_load_dwordx2 %0, %2, %4 sc1\n\tglobal_load_dword %1, %3, %4 sc1" : "=&v"(hp), "=&v"(zf) : "v"(hoff), "v"(zoff), "s"(ws) : "memory")
  GH();
  { const u32 o = (hq * 56 + hcol + (l >> 5)) * 2; asm volatile("global_load_ushort %0, %1, %2" : "=&v"(b1v) : "v"(o), "s"(b1) : "memory"); }
  STAMP(6);
  asm volatile("s_waitcnt vmcnt(11)" : "+v"(qa), "+v"(qb) :: "memory");
  STAMP(7);
  u32 spins = 0;
  for (;;) {
    const u32 bad = ((qa[0] ^ T0) | (qa[1] ^ T1) | (qa[2] ^ T2) | (qa[3] ^ T3) | (qb[0] ^ T0) | (qb[1] ^ T1) | (qb[2] ^ T2) | (qb[3] ^ T3)) & 255;
    if (__builtin_expect(__all(bad == 0), 1) || ++spins > SPIN_CAP) break;
    G1();
    asm volatile("s_waitcnt vmcnt(0)" : "+v"(qa), "+v"(qb) :: "memory");
  }
  {
    const float s0 = u2f(qa[0]) + u2f(qb[0]), s1 = u2f(qa[1]) + u2f(qb[1]), s2 = u2f(qa[2]) + u2f(qb[2]), s3 = u2f(qa[3]) + u2f(qb[3]);
    const float a = sw32_add(s0, s2), c = sw32_add(s1, s3);
    *(LAS u32*)(size_t)(L_Q + w * 320 + (l & 31) * 8 + (l >> 5) * 4) = pkbf(a, c);
  }
  {
    const u32 vo = grow * 1024 + gchk;
    const char* p2 = w2 + hq * (56 * 1024) + cg * 128;
    DMA(L_W2 + w * 1024, vo, p2, "nt");
    DMA(L_W2 + w * 1024 + 4096, w < 3 ? vo + 32 * 1024 : 0, p2, "nt");
    const char* p = wo + hq * (128 * 1024) + cg * 128;
#pragma unroll
    for (int i = 0; i < 4; ++i) DMA(L_WO + w * 1024 + i * 4096, vo + i * 32 * 1024, p, "nt");
  }
  BAR();
  STAMP(8);
  const u32 hd = n >> 2;
  float M;
  {
    f32x4 s0 = zero4, s1 = zero4;
    const u32 ka = L_K + (32 * w + n) * 256 + (g ^ n) * 16;
#pragma unroll
    for (int kk = 0; kk < 4; ++kk) {
      const u32x4 qf = ldsr128(L_Q + hd * 320 + g * 16 + kk * 64);
      s0 = mfma(ldsr128(ka ^ (kk * 64)), qf, s0);
      s1 = mfma(ldsr128((ka ^ (kk * 64)) + 4096), qf, s1);
    }
    float m = fmaxf(fmaxf(fmaxf(s0[0], s0[1]), fmaxf(s0[2], s0[3])), fmaxf(fmaxf(s1[0], s1[1]), fmaxf(s1[2], s1[3])));
    m = sw16_max(m); m = sw32_max(m);
    if ((l & 0x33) == 0) *(LAS float*)(size_t)(L_M + hd * 16 + w * 4) = m;
    BAR();
    const u32x4 mm = ldsr128(L_M + hd * 16);
    constexpr float C2 = 0.08838834764831845f * 1.4426950408889634f;
    M = fmaxf(fmaxf(u2f(mm[0]), u2f(mm[1])), fmaxf(u2f(mm[2]), u2f(mm[3]))) * C2;
    u32x4 pf;
    pf[0] = pkbf(__builtin_amdgcn_exp2f(fmaf(s0[0], C2, -M)), __builtin_amdgcn_exp2f(fmaf(s0[1], C2, -M)));
    pf[1] = pkbf(__builtin_amdgcn_exp2f(fmaf(s0[2], C2, -M)), __builtin_amdgcn_exp2f(fmaf(s0[3], C2, -M)));
    pf[2] = pkbf(__builtin_amdgcn_exp2f(fmaf(s1[0], C2, -M)), __builtin_amdgcn_exp2f(fmaf(s1[1], C2, -M)));
    pf[3] = pkbf(__builtin_amdgcn_exp2f(fmaf(s1[2], C2, -M)), __builtin_amdgcn_exp2f(fmaf(s1[3], C2, -M)));
    if (p4 == 0) *(LAS u32x4*)(size_t)(L_P + w * 256 + g * 64 + hd * 16) = pf;
  }
  STAMP(9);
  asm volatile("s_waitcnt vmcnt(6) lgkmcnt(0)\n\ts_barrier" : "+v"(hp), "+v"(zf), "+v"(b1v) :: "memory");
  STAMP(10);
  {
    f32x4 d0 = zero4, d1 = zero4, dl = zero4;
    const u32 va = L_V + (4 * g + q4) * 256 + ((2 * w) ^ (4 * (g & 1) + q4)) * 32 + p4 * 8;
    const u32x4 ones = {0x3F803F80u, 0x3F803F80u, 0x3F803F80u, 0x3F803F80u};
#pragma unroll
    for (int kk = 0; kk < 4; ++kk) {
      const u32x4 pf = ldsr128(L_P + kk * 256 + g * 64 + hd * 16);
      d0 = mfma(pf, ldstr2(va + kk * 8192, va + kk * 8192 + 4096), d0);
      d1 = mfma(pf, ldstr2((va ^ 32) + kk * 8192, (va ^ 32) + kk * 8192 + 4096), d1);
      dl = mfma(ones, pf, dl);
    }
    const u32 ta = (n & 1) ? T2 : T0, tb = (n & 1) ? T3 : T1;
    const u32x2 v = {tagf(d0[0], ta), tagf(d1[0], tb)};
    const u32 o = W_OP + (((h * 4 + g) * 32 + s) * 128 + w * 32 + n * 2) * 4;
    asm volatile("global_store_dwordx2 %0, %1, %2 sc1" :: "v"(o), "v"(v), "s"(ws) : "memory");
    if (w == 0) {
      const u32 mv = tagf((n & 1) ? dl[0] : M, Tn), mo = W_ML + (b * 16 + n) * 4;
      if (l < 16) asm volatile("global_store_dword %0, %1, %2 sc1" :: "v"(mo), "v"(mv), "s"(ws) : "memory");
    }
  }
  STAMP(11);
  {
    const u32 ta = (l & 1) ? T2 : T0, tb = (l & 1) ? T3 : T1;
    const u32 tz = (w * 16 + (l & 7) * 2 < 56) ? ta : T0, ty = (w * 16 + (l & 7) * 2 < 56) ? tb : T1;
    for (;;) {
      const u32 bad = ((hp[0] ^ tz) | (hp[1] ^ ty) | (zf ^ Tn)) & 255;
      if (__builtin_expect(__all(bad == 0), 1) || ++spins > SPIN_CAP) break;
      GH();
      asm volatile("s_waitcnt vmcnt(0)" : "+v"(hp), "+v"(zf) :: "memory");
    }
    float hv = sw32_add(u2f(hp[0]), u2f(hp[1]));
    hv = sw16_add(hv, hv); hv += dpp<ROR8>(hv);
    hv += u2f(b1v << 16);
    const float u = __builtin_amdgcn_exp2f(-1.4426950408889634f * fabsf(hv));
    const float num = hv > 0.f ? fmaf(2.f, u, 1.f) : u * (u + 2.f);
    const float den = num + (hv > 0.f ? 2.f * u * u : 2.f);
    float act = hv * num * __builtin_amdgcn_rcpf(den);
    if (w * 16 + (l & 7) * 2 >= 56) act = 0.f;
    if ((l & 0x18) == 0) *(LAS unsigned short*)(size_t)(L_A + (w * 16 + (l & 7) * 2 + (l >> 5)) * 2) = (unsigned short)pkbf(act, act);
  }
  if (SLEEP2) __builtin_amdgcn_s_sleep(SLEEP2);
  STAMP(12);
  {
    u32x4 o0, o1, o2, o3, ml;
    const u32 oo = W_OP + ((hq * 32 + (l >> 3)) * 128 + w * 32 + (l & 7) * 4) * 4;
    const u32 mo = W_ML + ((((l >> 3) + 8 * (l & 3)) * 8 + h) * 16 + ks * 4) * 4;
    for (;;) {
      asm volatile("global_load_dwordx4 %4, %6, %7 sc1\n\t"
                   "global_load_dwordx4 %0, %5, %7 offset:-4096 sc1\n\tglobal_load_dwordx4 %1, %5, %7 sc1\n\t"
                   "global_load_dwordx4 %2, %8, %7 offset:-4096 sc1\n\tglobal_load_dwordx4 %3, %8, %7 sc1\n\t"
                   "s_waitcnt vmcnt(0)"
                   : "=&v"(o0), "=&v"(o1), "=&v"(o2), "=&v"(o3), "=&v"(ml) : "v"(oo + 4096), "v"(mo), "s"(ws), "v"(oo + 12288) : "memory");
      const u32 b0 = (o0[0] ^ T0) | (o1[0] ^ T0) | (o2[0] ^ T0) | (o3[0] ^ T0) | (ml[0] ^ T0);
      const u32 b1_ = (o0[1] ^ T1) | (o1[1] ^ T1) | (o2[1] ^ T1) | (o3[1] ^ T1) | (ml[1] ^ T1);
      const u32 b2 = (o0[2] ^ T2) | (o1[2] ^ T2) | (o2[2] ^ T2) | (o3[2] ^ T2) | (ml[2] ^ T2);
      const u32 b3 = (o0[3] ^ T3) | (o1[3] ^ T3) | (o2[3] ^ T3) | (o3[3] ^ T3) | (ml[3] ^ T3);
      if (__builtin_expect(__all(((b0 | b1_ | b2 | b3) & 255) == 0), 1) || ++spins > SPIN_CAP) break;
    }
    STAMP(13);
    const float m = u2f(ml[0]);
    float mx = fmaxf(m, dpp<XOR1>(m)); mx = fmaxf(mx, dpp<XOR2>(mx)); mx = fmaxf(mx, dpp<ROR8>(mx)); mx = sw16_max(mx); mx = sw32_max(mx);
    const float wt = __builtin_amdgcn_exp2f(m - mx);
    float L = wt * u2f(ml[1]);
    L += dpp<XOR1>(L); L += dpp<XOR2>(L); L += dpp<ROR8>(L); L = sw16_add(L, L); L = sw32_add(L, L);
    const float w0 = dpp<0x00>(wt), w1_ = dpp<0x55>(wt), w2_ = dpp<0xAA>(wt), w3 = dpp<0xFF>(wt);
    float r[4];
#pragma unroll
    for (int j = 0; j < 4; ++j) r[j] = fmaf(w3, u2f(o3[j]), fmaf(w2_, u2f(o2[j]), fmaf(w1_, u2f(o1[j]), w0 * u2f(o0[j]))));
    const float a = sw32_add(r[0], r[2]), c = sw32_add(r[1], r[3]);
    float ov = sw16_add(a, c);
    ov += dpp<ROR8>(ov);
    ov *= __builtin_amdgcn_rcpf(L);
    if ((l & 8) == 0) *(LAS unsigned short*)(size_t)(L_O + (w * 32 + 16 * (g & 1) + 2 * (l & 7) + (g >> 1)) * 2) = (unsigned short)pkbf(ov, ov);
  }
  BAR();
  {
    f32x4 da = zero4, db = zero4;
    const u32 gt2 = gtr + L_W2;
#pragma unroll
    for (int kk = 0; kk < 4; ++kk) da = mfma(ldsr128(L_O + g * 16 + kk * 64), ldstr2(gt2 + (L_WO - L_W2) + kk * 4096, gt2 + (L_WO - L_W2) + kk * 4096 + 512), da);
#pragma unroll
    for (int kk = 0; kk < 2; ++kk) db = mfma(ldsr128(L_A + g * 16 + kk * 64), ldstr2(gt2 + kk * 4096, gt2 + kk * 4096 + 512), db);
    const float res = fmaf(gate, da[0], (1.f - gate) * db[0]);
    const bool inr = fabsf(res) < 1000.f;
    const u64 bits = __builtin_bit_cast(u64, (double)res + 66560.0);
    const u32 lo = inr ? (u32)bits : 0u, hi = inr ? (u32)(bits >> 32) + ((1u << 26) - 0x40F00000u) : ((1u << 26) + (1u << 20) + (1u << 14));
    const u64 word = ((u64)hi << 32) | lo;
    const u32 fo = W_FP + ((cg * 32 + hq) * 64 + w * 16 + n) * 8;
    STAMP(14);
    if (l < 16) {
      u64 old; const u32x2 fv = {f2u(res), E};
      asm volatile("global_atomic_add_x2 %0, %1, %2, %3 sc0 sc1\n\tglobal_store_dwordx2 %4, %5, %3 sc1\n\ts_waitcnt vmcnt(1)"
                   : "=&v"(old) : "v"(accoff), "v"(word), "s"(ws), "v"(fo), "v"(fv) : "memory");
      const u64 tot = old + word;
      const u32 th = (u32)(tot >> 32);
      if ((th >> 26) == 32) {
        float sum = (float)(__builtin_bit_cast(double, ((u64)((th & 0xFFFFFu) | 0x40F00000u) << 32) | (u32)tot) - 98304.0);
        if (__builtin_expect((th & 0x03F00000u) != 0 || fmaxf(fabsf(sum), fabsf(res)) < 9.5367431640625e-07f, 0)) {
          sum = 0.f;
          for (u32 j = 0; j < 32; ++j) {
            u32x2 pv; const u32 po = W_FP + ((cg * 32 + j) * 64 + w * 16 + n) * 8;
            do { asm volatile("global_load_dwordx2 %0, %1, %2 sc1\n\ts_waitcnt vmcnt(0)" : "=&v"(pv) : "v"(po), "s"(ws) : "memory"); } while (pv[1] != E && ++spins <= SPIN_CAP);
            sum += u2f(pv[0]);
          }
        }
        const u32 col = cg * 64 + w * 16 + n;
        *(unsigned short*)(out + col * 2) = (unsigned short)pkbf(sum, sum);
        if (col == 0) __hip_atomic_store((u32*)ws, E, __ATOMIC_RELAXED, __HIP_MEMORY_SCOPE_AGENT);
      }
    }
  }
}

extern "C" void kernel_launch(void* const* d_in, const int* in_sizes, int n_in, void* d_out, int out_size, void* d_ws, size_t ws_size, hipStream_t stream) {
  gm_kernel<<<256, 256, 0, stream>>>((const char*)d_in[3], (const char*)d_in[6], (const char*)d_in[1], (const char*)d_in[0], (char*)d_ws, (const char*)d_in[9],
                                     (const char*)d_in[2], (const char*)d_in[4], (const char*)d_in[8], (const char*)d_in[7], (char*)d_out);
}
```

```cpp
#define AMPA 0
#define AMPX 16
#include <hip/hip_runtime.h>
#include <stdint.h>

typedef unsigned u32;
typedef unsigned long long u64;
typedef __attribute__((ext_vector_type(2))) unsigned u32x2;
typedef __attribute__((ext_vector_type(4))) unsigned u32x4;
typedef __attribute__((ext_vector_type(4))) float f32x4;
typedef __attribute__((ext_vector_type(4))) short s16x4;
typedef __attribute__((ext_vector_type(8))) __bf16 bf16x8;
typedef __attribute__((ext_vector_type(2))) __bf16 bf16x2;
#define LAS __attribute__((address_space(3)))
#define CAS __attribute__((address_space(4)))

constexpr u32 L_WQ = 0, L_W1 = 16384, L_K = 24576, L_V = 57344, L_W2 = 90112, L_WO = 98304;
constexpr u32 L_X = 114688, L_GW = 115712, L_JUNK = 116736, L_Q = 117760, L_P = 119040, L_M = 120064, L_O = 120128, L_A = 120384, L_END = 120512;
constexpr u32 W_ACC = 4096;
constexpr u32 W_QP = 16384;
constexpr u32 W_HP = W_QP + 65536;
constexpr u32 W_ML = W_HP + 73728;
constexpr u32 W_FP = W_ML + 16384;
constexpr u32 W_OP = W_FP + 131072;

#define STAMP(i) do { if ((i) == AMPA || (i) == AMPX) { __builtin_amdgcn_sched_barrier(0); asm volatile("s_memrealtime %0\n\ts_waitcnt lgkmcnt(0)" : "=s"(ts[(i) == AMPX]) :: "memory"); __builtin_amdgcn_sched_barrier(0); } } while (0)
#define SLEEP1 0
#define SLEEP2 0
#define KWAIT 8

#define DMA(ldsaddr, voff, base, pol) asm volatile("s_mov_b32 m0, %0\n\ts_nop 0\n\tglobal_load_lds_dwordx4 %1, %2 " pol :: "s"(ldsaddr), "v"(voff), "s"(base) : "memory")
#define WAITV(n) asm volatile("s_waitcnt vmcnt(" #n ")" ::: "memory")
#define WAITV_(n) WAITV(n)
#define BARV(n) asm volatile("s_waitcnt vmcnt(" #n ") lgkmcnt(0)\n\ts_barrier" ::: "memory")
#define BAR() asm volatile("s_waitcnt lgkmcnt(0)\n\ts_barrier" ::: "memory")

__device__ __forceinline__ f32x4 mfma(u32x4 a, u32x4 b, f32x4 c) {
  return __builtin_amdgcn_mfma_f32_16x16x32_bf16(__builtin_bit_cast(bf16x8, a), __builtin_bit_cast(bf16x8, b), c, 0, 0, 0);
}
__device__ __forceinline__ u32x4 ldsr128(u32 a) { return *(const LAS u32x4*)(size_t)a; }
__device__ __forceinline__ u32x2 ldstr(u32 a) { return __builtin_bit_cast(u32x2, __builtin_amdgcn_ds_read_tr16_b64_v4i16((LAS s16x4*)(size_t)a)); }
__device__ __forceinline__ u32x4 ldstr2(u32 a, u32 b) { u32x2 x = ldstr(a), y = ldstr(b); return u32x4{x[0], x[1], y[0], y[1]}; }
__device__ __forceinline__ u32 pkbf(float lo, float hi) { bf16x2 v = {(__bf16)lo, (__bf16)hi}; return __builtin_bit_cast(u32, v); }
__device__ __forceinline__ u32 f2u(float x) { return __float_as_uint(x); }
__device__ __forceinline__ float u2f(u32 x) { return __uint_as_float(x); }
__device__ __forceinline__ u32 tagf(float v, u32 t) { return (f2u(v) & 0xFFFFFF00u) | t; }
template <int CTRL> __device__ __forceinline__ float dpp(float x) { return u2f(__builtin_amdgcn_mov_dpp(f2u(x), CTRL, 0xf, 0xf, true)); }
constexpr int XOR1 = 0xB1, XOR2 = 0x4E, ROR4 = 0x124, ROR8 = 0x128;
__device__ __forceinline__ float sw32_add(float a, float b) { auto r = __builtin_amdgcn_permlane32_swap(f2u(a), f2u(b), false, false); return u2f(r[0]) + u2f(r[1]); }
__device__ __forceinline__ float sw16_add(float a, float b) { auto r = __builtin_amdgcn_permlane16_swap(f2u(a), f2u(b), false, false); return u2f(r[0]) + u2f(r[1]); }
__device__ __forceinline__ float sw32_max(float a) { auto r = __builtin_amdgcn_permlane32_swap(f2u(a), f2u(a), false, false); return fmaxf(u2f(r[0]), u2f(r[1])); }
__device__ __forceinline__ float sw16_max(float a) { auto r = __builtin_amdgcn_permlane16_swap(f2u(a), f2u(a), false, false); return fmaxf(u2f(r[0]), u2f(r[1])); }
__device__ __forceinline__ float dot2(u32 a, u32 b, float c) { return __builtin_amdgcn_fdot2_f32_bf16(__builtin_bit_cast(bf16x2, a), __builtin_bit_cast(bf16x2, b), c, false); }
constexpr u32 SPIN_CAP = 1u << 20;

__global__ void __launch_bounds__(256, 1)
gm_kernel(const char* wq, const char* w1, const char* kc, const char* x, char* ws, const char* gw, const char* vc,
          const char* wo, const char* w2, const char* b1, char* out) {
  __shared__ __attribute__((aligned(1024))) char lds_[L_END];
  asm volatile("" :: "v"((LAS char*)lds_) : "memory");
  u64 ts[2];
  STAMP(0);
  asm volatile("" :: "s"(wq), "s"(ws));
  STAMP(16);
  const u32 b = blockIdx.x, h = b & 7, s = b >> 3, cg = s & 7, ks = s >> 3, hq = h * 4 + ks;
  const u32 t = threadIdx.x, l = t & 63, w = __builtin_amdgcn_readfirstlane(t >> 6);
  const u32 n = l & 15, g = l >> 4;
  const u32 accoff = W_ACC + (cg * 4 + w) * 256 + n * 8;

  if (h == 0 && ks == 0) {
    if (l < 16) { u64 z = 0; asm volatile("global_store_dwordx2 %0, %1, %2 sc1" :: "v"(accoff), "v"(z), "s"(ws) : "memory"); }
  }
  const u32 grow = 8 * w + (l >> 3);
  const u32 gchk = ((l & 7) ^ ((((l >> 4) & 1) | ((w & 1) << 1)) << 1)) * 16;
  {
    const char* p = wq + (size_t)ks * (128 * 8192) + h * 1024 + cg * 128;
    const u32 vo = grow * 8192 + gchk;
#pragma unroll
    for (int i = 0; i < 4; ++i) DMA(L_WQ + w * 1024 + i * 4096, vo + i * 32 * 8192, p, "nt");
  }
  {
    u64 pc; asm volatile("s_getpc_b64 %0" : "=s"(pc));
    const char* p = w == 0 ? x : w == 1 ? gw : (const char*)(pc & ~127ull) + (w - 2) * 3584;
    const u32 vo = w < 2 ? l * 16 : (l < 28 ? l : 0) * 128;
    DMA(L_X + (w < 2 ? w : 2) * 1024, vo, p, "");
  }
  const u32 cg1 = b % 28, ks8 = b / 28;
  {
    const bool on = b < 224;
    const char* p = w1 + (on ? ks8 * (64 * 3584) + cg1 * 128 : 0);
    const u32 vo = on ? grow * 3584 + gchk : 0, st = on ? 32 * 3584 : 0;
#pragma unroll
    for (int i = 0; i < 2; ++i) DMA(L_W1 + w * 1024 + i * 4096, vo + i * st, p, "nt");
  }
  const size_t kvoff = ((size_t)h * 65536 + 61440) * 256 + s * 4096;
  const u32 krow = 4 * w + g;
  {
    const char* p = kc + kvoff;
    const u32 vo = krow * 256 + (n ^ krow) * 16;
#pragma unroll
    for (int i = 0; i < 8; ++i) DMA(L_K + w * 1024 + i * 4096, vo + i * 131072, p, "nt");
  }
  const u32 E = *(const CAS u32*)ws + 1;
  const u32 T0 = E & 255, T1 = (E + 67) & 255, T2 = (E + 134) & 255, T3 = (E + 201) & 255;
  const u32 Tn = (E + 67 * (l & 3)) & 255;
  asm volatile("" :: "s"(E));
  STAMP(1);

  const u32 q4 = (l >> 2) & 3, p4 = l & 3;
  const u32 gtr = (8 * g + q4) * 128 + (w ^ ((q4 >> 1) | ((g & 1) << 1))) * 32 + p4 * 8;
  const f32x4 zero4 = {0.f, 0.f, 0.f, 0.f};

  BARV(10);
  STAMP(2);
  {
    f32x4 acc = zero4;
    const u32 xa = L_X + ks * 256 + g * 16;
#pragma unroll
    for (int kk = 0; kk < 4; ++kk) acc = mfma(ldsr128(xa + kk * 64), ldstr2(gtr + L_WQ + kk * 4096, gtr + L_WQ + kk * 4096 + 512), acc);
    const u32 v = tagf(acc[0], Tn), o = W_QP + ((h * 4 + ks) * 512 + cg * 64 + w * 16 + n) * 4;
    if (l < 16) asm volatile("global_store_dword %0, %1, %2 sc1" :: "v"(o), "v"(v), "s"(ws) : "memory");
  }
  STAMP(3);
  BARV(9);
  {
    f32x4 acc = zero4;
    const u32 xa = L_X + (ks8 & 7) * 128 + g * 16;
#pragma unroll
    for (int kk = 0; kk < 2; ++kk) acc = mfma(ldsr128(xa + kk * 64), ldstr2(gtr + L_W1 + kk * 4096, gtr + L_W1 + kk * 4096 + 512), acc);
    const u32 v = tagf(acc[0], Tn), o = W_HP + (ks8 * 1792 + cg1 * 64 + w * 16 + n) * 4;
    if (l < 16) asm volatile("global_store_dword %0, %1, %2 sc1" :: "v"(o), "v"(v), "s"(ws) : "memory");
  }
  float gate;
  {
    const u32x4 a = ldsr128(L_X + l * 16), c = ldsr128(L_GW + l * 16);
    float z = dot2(a[0], c[0], 0.f); z = dot2(a[1], c[1], z); z = dot2(a[2], c[2], z); z = dot2(a[3], c[3], z);
    z += dpp<XOR1>(z); z += dpp<XOR2>(z); z += dpp<ROR4>(z); z += dpp<ROR8>(z);
    z = sw16_add(z, z); z = sw32_add(z, z);
    gate = __builtin_amdgcn_rcpf(1.f + __builtin_amdgcn_exp2f(-1.4426950408889634f * z));
    asm volatile("" : "+v"(gate));
  }
  STAMP(4);
  WAITV_(KWAIT);
  if (SLEEP1) __builtin_amdgcn_s_sleep(SLEEP1);
  STAMP(5);
  u32x4 qa, qb;
  const u32 qoff = W_QP + ((h * 4 + 2 * (l >> 5)) * 512 + w * 128 + (l & 31) * 4) * 4;
#define G1() asm volatile("global_load_dwordx4 %0, %2, %3 sc1\n\tglobal_load_dwordx4 %1, %2, %3 offset:2048 sc1" : "=&v"(qa), "=&v"(qb) : "v"(qoff), "s"(ws) : "memory")
  G1();
  {
    const char* p = vc + kvoff;
    const u32 vo = krow * 256 + (n ^ ((krow & 7) << 1)) * 16;
#pragma unroll
    for (int i = 0; i < 8; ++i) DMA(L_V + w * 1024 + i * 4096, vo + i * 131072, p, "nt");
  }
  u32x2 hp; u32 zf, b1v;
  const u32 hcol = (w * 16 + (l & 7) * 2 < 56) ? w * 16 + (l & 7) * 2 : 0;
  const u32 hoff = W_HP + ((l >> 3) * 1792 + hq * 56 + hcol) * 4;
  const u32 zoff = W_QP + (cg * 64 + n) * 4;
#define GH() asm volatile("global_load_dwordx2 %0, %2, %4 sc1\n\tglobal_load_dword %1, %3, %4 sc1" : "=&v"(hp), "=&v"(zf) : "v"(hoff), "v"(zoff), "s"(ws) : "memory")
  GH();
  { const u32 o = (hq * 56 + hcol + (l >> 5)) * 2; asm volatile("global_load_ushort %0, %1, %2" : "=&v"(b1v) : "v"(o), "s"(b1) : "memory"); }
  STAMP(6);
  asm volatile("s_waitcnt vmcnt(11)" : "+v"(qa), "+v"(qb) :: "memory");
  STAMP(7);
  u32 spins = 0;
  for (;;) {
    const u32 bad = ((qa[0] ^ T0) | (qa[1] ^ T1) | (qa[2] ^ T2) | (qa[3] ^ T3) | (qb[0] ^ T0) | (qb[1] ^ T1) | (qb[2] ^ T2) | (qb[3] ^ T3)) & 255;
    if (__builtin_expect(__all(bad == 0), 1) || ++spins > SPIN_CAP) break;
    G1();
    asm volatile("s_waitcnt vmcnt(0)" : "+v"(qa), "+v"(qb) :: "memory");
  }
  {
    const float s0 = u2f(qa[0]) + u2f(qb[0]), s1 = u2f(qa[1]) + u2f(qb[1]), s2 = u2f(qa[2]) + u2f(qb[2]), s3 = u2f(qa[3]) + u2f(qb[3]);
    const float a = sw32_add(s0, s2), c = sw32_add(s1, s3);
    *(LAS u32*)(size_t)(L_Q + w * 320 + (l & 31) * 8 + (l >> 5) * 4) = pkbf(a, c);
  }
  {
    const u32 vo = grow * 1024 + gchk;
    const char* p2 = w2 + hq * (56 * 1024) + cg * 128;
    DMA(L_W2 + w * 1024, vo, p2, "nt");
    DMA(L_W2 + w * 1024 + 4096, w < 3 ? vo + 32 * 1024 : 0, p2, "nt");
    const char* p = wo + hq * (128 * 1024) + cg * 128;
#pragma unroll
    for (int i = 0; i < 4; ++i) DMA(L_WO + w * 1024 + i * 4096, vo + i * 32 * 1024, p, "nt");
  }
  BAR();
  STAMP(8);
  const u32 hd = n >> 2;
  float M;
  {
    f32x4 s0 = zero4, s1 = zero4;
    const u32 ka = L_K + (32 * w + n) * 256 + (g ^ n) * 16;
#pragma unroll
    for (int kk = 0; kk < 4; ++kk) {
      const u32x4 qf = ldsr128(L_Q + hd * 320 + g * 16 + kk * 64);
      s0 = mfma(ldsr128(ka ^ (kk * 64)), qf, s0);
      s1 = mfma(ldsr128((ka ^ (kk * 64)) + 4096), qf, s1);
    }
    float m = fmaxf(fmaxf(fmaxf(s0[0], s0[1]), fmaxf(s0[2], s0[3])), fmaxf(fmaxf(s1[0], s1[1]), fmaxf(s1[2], s1[3])));
    m = sw16_max(m); m = sw32_max(m);
    if ((l & 0x33) == 0) *(LAS float*)(size_t)(L_M + hd * 16 + w * 4) = m;
    BAR();
    const u32x4 mm = ldsr128(L_M + hd * 16);
    constexpr float C2 = 0.08838834764831845f * 1.4426950408889634f;
    M = fmaxf(fmaxf(u2f(mm[0]), u2f(mm[1])), fmaxf(u2f(mm[2]), u2f(mm[3]))) * C2;
    u32x4 pf;
    pf[0] = pkbf(__builtin_amdgcn_exp2f(fmaf(s0[0], C2, -M)), __builtin_amdgcn_exp2f(fmaf(s0[1], C2, -M)));
    pf[1] = pkbf(__builtin_amdgcn_exp2f(fmaf(s0[2], C2, -M)), __builtin_amdgcn_exp2f(fmaf(s0[3], C2, -M)));
    pf[2] = pkbf(__builtin_amdgcn_exp2f(fmaf(s1[0], C2, -M)), __builtin_amdgcn_exp2f(fmaf(s1[1], C2, -M)));
    pf[3] = pkbf(__builtin_amdgcn_exp2f(fmaf(s1[2], C2, -M)), __builtin_amdgcn_exp2f(fmaf(s1[3], C2, -M)));
    if (p4 == 0) *(LAS u32x4*)(size_t)(L_P + w * 256 + g * 64 + hd * 16) = pf;
  }
  STAMP(9);
  asm volatile("s_waitcnt vmcnt(6) lgkmcnt(0)\n\ts_barrier" : "+v"(hp), "+v"(zf), "+v"(b1v) :: "memory");
  STAMP(10);
  {
    f32x4 d0 = zero4, d1 = zero4, dl = zero4;
    const u32 va = L_V + (4 * g + q4) * 256 + ((2 * w) ^ (4 * (g & 1) + q4)) * 32 + p4 * 8;
    const u32x4 ones = {0x3F803F80u, 0x3F803F80u, 0x3F803F80u, 0x3F803F80u};
#pragma unroll
    for (int kk = 0; kk < 4; ++kk) {
      const u32x4 pf = ldsr128(L_P + kk * 256 + g * 64 + hd * 16);
      d0 = mfma(pf, ldstr2(va + kk * 8192, va + kk * 8192 + 4096), d0);
      d1 = mfma(pf, ldstr2((va ^ 32) + kk * 8192, (va ^ 32) + kk * 8192 + 4096), d1);
      dl = mfma(ones, pf, dl);
    }
    const u32 ta = (n & 1) ? T2 : T0, tb = (n & 1) ? T3 : T1;
    const u32x2 v = {tagf(d0[0], ta), tagf(d1[0], tb)};
    const u32 o = W_OP + (((h * 4 + g) * 32 + s) * 128 + w * 32 + n * 2) * 4;
    asm volatile("global_store_dwordx2 %0, %1, %2 sc1" :: "v"(o), "v"(v), "s"(ws) : "memory");
    if (w == 0) {
      const u32 mv = tagf((n & 1) ? dl[0] : M, Tn), mo = W_ML + (b * 16 + n) * 4;
      if (l < 16) asm volatile("global_store_dword %0, %1, %2 sc1" :: "v"(mo), "v"(mv), "s"(ws) : "memory");
    }
  }
  STAMP(11);
  {
    const u32 ta = (l & 1) ? T2 : T0, tb = (l & 1) ? T3 : T1;
    const u32 tz = (w * 16 + (l & 7) * 2 < 56) ? ta : T0, ty = (w * 16 + (l & 7) * 2 < 56) ? tb : T1;
    for (;;) {
      const u32 bad = ((hp[0] ^ tz) | (hp[1] ^ ty) | (zf ^ Tn)) & 255;
      if (__builtin_expect(__all(bad == 0), 1) || ++spins > SPIN_CAP) break;
      GH();
      asm volatile("s_waitcnt vmcnt(0)" : "+v"(hp), "+v"(zf) :: "memory");
    }
    float hv = sw32_add(u2f(hp[0]), u2f(hp[1]));
    hv = sw16_add(hv, hv); hv += dpp<ROR8>(hv);
    hv += u2f(b1v << 16);
    const float u = __builtin_amdgcn_exp2f(-1.4426950408889634f * fabsf(hv));
    const float num = hv > 0.f ? fmaf(2.f, u, 1.f) : u * (u + 2.f);
    const float den = num + (hv > 0.f ? 2.f * u * u : 2.f);
    float act = hv * num * __builtin_amdgcn_rcpf(den);
    if (w * 16 + (l & 7) * 2 >= 56) act = 0.f;
    if ((l & 0x18) == 0) *(LAS unsigned short*)(size_t)(L_A + (w * 16 + (l & 7) * 2 + (l >> 5)) * 2) = (unsigned short)pkbf(act, act);
  }
  if (SLEEP2) __builtin_amdgcn_s_sleep(SLEEP2);
  STAMP(12);
  {
    u32x4 o0, o1, o2, o3, ml;
    const u32 oo = W_OP + ((hq * 32 + (l >> 3)) * 128 + w * 32 + (l & 7) * 4) * 4;
    const u32 mo = W_ML + ((((l >> 3) + 8 * (l & 3)) * 8 + h) * 16 + ks * 4) * 4;
    for (;;) {
      asm volatile("global_load_dwordx4 %4, %6, %7 sc1\n\t"
                   "global_load_dwordx4 %0, %5, %7 offset:-4096 sc1\n\tglobal_load_dwordx4 %1, %5, %7 sc1\n\t"
                   "global_load_dwordx4 %2, %8, %7 offset:-4096 sc1\n\tglobal_load_dwordx4 %3, %8, %7 sc1\n\t"
                   "s_waitcnt vmcnt(0)"
                   : "=&v"(o0), "=&v"(o1), "=&v"(o2), "=&v"(o3), "=&v"(ml) : "v"(oo + 4096), "v"(mo), "s"(ws), "v"(oo + 12288) : "memory");
      const u32 b0 = (o0[0] ^ T0) | (o1[0] ^ T0) | (o2[0] ^ T0) | (o3[0] ^ T0) | (ml[0] ^ T0);
      const u32 b1_ = (o0[1] ^ T1) | (o1[1] ^ T1) | (o2[1] ^ T1) | (o3[1] ^ T1) | (ml[1] ^ T1);
      const u32 b2 = (o0[2] ^ T2) | (o1[2] ^ T2) | (o2[2] ^ T2) | (o3[2] ^ T2) | (ml[2] ^ T2);
      const u32 b3 = (o0[3] ^ T3) | (o1[3] ^ T3) | (o2[3] ^ T3) | (o3[3] ^ T3) | (ml[3] ^ T3);
      if (__builtin_expect(__all(((b0 | b1_ | b2 | b3) & 255) == 0), 1) || ++spins > SPIN_CAP) break;
    }
    STAMP(13);
    const float m = u2f(ml[0]);
    float mx = fmaxf(m, dpp<XOR1>(m)); mx = fmaxf(mx, dpp<XOR2>(mx)); mx = fmaxf(mx, dpp<ROR8>(mx)); mx = sw16_max(mx); mx = sw32_max(mx);
    const float wt = __builtin_amdgcn_exp2f(m - mx);
    float L = wt * u2f(ml[1]);
    L += dpp<XOR1>(L); L += dpp<XOR2>(L); L += dpp<ROR8>(L); L = sw16_add(L, L); L = sw32_add(L, L);
    const float w0 = dpp<0x00>(wt), w1_ = dpp<0x55>(wt), w2_ = dpp<0xAA>(wt), w3 = dpp<0xFF>(wt);
    float r[4];
#pragma unroll
    for (int j = 0; j < 4; ++j) r[j] = fmaf(w3, u2f(o3[j]), fmaf(w2_, u2f(o2[j]), fmaf(w1_, u2f(o1[j]), w0 * u2f(o0[j]))));
    const float a = sw32_add(r[0], r[2]), c = sw32_add(r[1], r[3]);
    float ov = sw16_add(a, c);
    ov += dpp<ROR8>(ov);
    ov *= __builtin_amdgcn_rcpf(L);
    if ((l & 8) == 0) *(LAS unsigned short*)(size_t)(L_O + (w * 32 + 16 * (g & 1) + 2 * (l & 7) + (g >> 1)) * 2) = (unsigned short)pkbf(ov, ov);
  }
  BAR();
  {
    f32x4 da = zero4, db = zero4;
    const u32 gt2 = gtr + L_W2;
#pragma unroll
    for (int kk = 0; kk < 4; ++kk) da = mfma(ldsr128(L_O + g * 16 + kk * 64), ldstr2(gt2 + (L_WO - L_W2) + kk * 4096, gt2 + (L_WO - L_W2) + kk * 4096 + 512), da);
#pragma unroll
    for (int kk = 0; kk < 2; ++kk) db = mfma(ldsr128(L_A + g * 16 + kk * 64), ldstr2(gt2 + kk * 4096, gt2 + kk * 4096 + 512), db);
    const float res = fmaf(gate, da[0], (1.f - gate) * db[0]);
    const bool inr = fabsf(res) < 1000.f;
    const u64 bits = __builtin_bit_cast(u64, (double)res + 66560.0);
    const u32 lo = inr ? (u32)bits : 0u, hi = inr ? (u32)(bits >> 32) + ((1u << 26) - 0x40F00000u) : ((1u << 26) + (1u << 20) + (1u << 14));
    const u64 word = ((u64)hi << 32) | lo;
    const u32 fo = W_FP + ((cg * 32 + hq) * 64 + w * 16 + n) * 8;
    STAMP(14);
    if (l < 16) {
      u64 old; const u32x2 fv = {f2u(res), E};
      asm volatile("global_atomic_add_x2 %0, %1, %2, %3 sc0 sc1\n\tglobal_store_dwordx2 %4, %5, %3 sc1\n\ts_waitcnt vmcnt(1)"
                   : "=&v"(old) : "v"(accoff), "v"(word), "s"(ws), "v"(fo), "v"(fv) : "memory");
      const u64 tot = old + word;
      const u32 th = (u32)(tot >> 32);
      if ((th >> 26) == 32) {
        float sum = (float)(__builtin_bit_cast(double, ((u64)((th & 0xFFFFFu) | 0x40F00000u) << 32) | (u32)tot) - 98304.0);
        if (__builtin_expect((th & 0x03F00000u) != 0 || fmaxf(fabsf(sum), fabsf(res)) < 9.5367431640625e-07f, 0)) {
          sum = 0.f;
          for (u32 j = 0; j < 32; ++j) {
            u32x2 pv; const u32 po = W_FP + ((cg * 32 + j) * 64 + w * 16 + n) * 8;
            do { asm volatile("global_load_dwordx2 %0, %1, %2 sc1\n\ts_waitcnt vmcnt(0)" : "=&v"(pv) : "v"(po), "s"(ws) : "memory"); } while (pv[1] != E && ++spins <= SPIN_CAP);
            sum += u2f(pv[0]);
          }
        }
        const u32 col = cg * 64 + w * 16 + n;
        *(unsigned short*)(out + col * 2) = (unsigned short)pkbf(sum, sum);
        if (col == 0) __hip_atomic_store((u32*)ws, E, __ATOMIC_RELAXED, __HIP_MEMORY_SCOPE_AGENT);
      }
    }
  }
  STAMP(15);
  { const u64 tgt = __builtin_amdgcn_s_memrealtime() + 10 * (ts[1] - ts[0]); while (__builtin_amdgcn_s_memrealtime() < tgt) __builtin_amdgcn_s_sleep(1); }
}

extern "C" void kernel_launch(void* const* d_in, const int* in_sizes, int n_in, void* d_out, int out_size, void* d_ws, size_t ws_size, hipStream_t stream) {
  gm_kernel<<<256, 256, 0, stream>>>((const char*)d_in[3], (const char*)d_in[6], (const char*)d_in[1], (const char*)d_in[0], (char*)d_ws, (const char*)d_in[9],
                                     (const char*)d_in[2], (const char*)d_in[4], (const char*)d_in[8], (const char*)d_in[7], (char*)d_out);
}
```

```cpp
#include <hip/hip_runtime.h>

__global__ void __launch_bounds__(384, 2)
gm_kernel(const char* wq, const char* x, const char* gw, const char* w1, const char* kc, char* ws, const char* vc,
          const char* wo, const char* w2, const char* b1, char* out) {
  __shared__ __attribute__((aligned(1024))) char lds[122880];
  asm volatile(
    "s_load_dwordx16 s[4:19], s[0:1], 0\n"
    "s_load_dwordx4 s[20:23], s[0:1], 64\n"
    "s_load_dwordx2 s[24:25], s[0:1], 80\n"
    "v_readfirstlane_b32 s34, v0\n"
    "s_getpc_b64 s[26:27]\n"
    ".Lpc0:\n"
    "v_lshlrev_b32 v5, 7, v0\n"
    "v_cmp_gt_u32 vcc, 59, v0\n"
    "s_cbranch_vccz .Lnopf\n"
    "s_mov_b64 exec, vcc\n"
    "global_load_dword v9, v5, s[26:27] offset:128\n"
    "s_mov_b64 exec, -1\n"
    ".Lnopf:\n"
    "s_lshr_b32 s34, s34, 6\n"
    "s_and_b32 s35, s2, 7\n"
    "s_lshr_b32 s36, s2, 3\n"
    "s_and_b32 s37, s36, 7\n"
    "s_lshr_b32 s38, s2, 6\n"
    "s_mul_i32 s40, s2, 2341\n"
    "s_lshr_b32 s40, s40, 16\n"
    "s_xor_b32 s41, s35, 1\n"
    "s_lshl_b32 s39, s41, 2\n"
    "s_add_u32 s39, s39, s38\n"
    "v_and_b32 v1, 63, v0\n"
    "v_mov_b32 v14, 0\n"
    "v_mov_b32 v15, 0\n"
    "v_mov_b32 v10, 1\n"
    "v_lshlrev_b32 v7, 2, v0\n"
    "ds_write_b32 v7, v14 offset:39936\n"
    "v_and_b32 v7, 60, v7\n"
    "ds_write_b32 v7, v14 offset:5376\n"
    "v_lshrrev_b32 v5, 3, v1\n"
    "v_lshlrev_b32 v7, 4, v1\n"
    "v_lshlrev_b32 v8, 1, v1\n"
    "v_and_b32 v6, 0x70, v7\n"
    "v_and_b32 v11, 0x20, v8\n"
    "v_xor_b32 v6, v6, v11\n"
    "s_lshl_b32 s60, s37, 7\n"
    "s_lshl_b32 s49, s38, 18\n"
    "s_lshl_b32 s50, s41, 10\n"
    "s_add_u32 s49, s49, s50\n"
    "s_add_u32 s49, s49, s60\n"
    "v_lshl_add_u32 v16, v5, 13, s49\n"
    "v_add_u32 v16, v16, v6\n"
    "v_xor_b32 v17, 64, v16\n"
    "v_add_u32 v18, 2095104, v16\n"
    "v_add_u32 v17, 1047552, v17\n"
    "v_add_u32 v19, 2095104, v17\n"
    "s_lshl_b32 s49, s35, 24\n"
    "s_lshl_b32 s50, s36, 10\n"
    "s_add_u32 s61, s49, s50\n"
    "s_lshl_b32 s50, s41, 19\n"
    "s_mul_i32 s43, s41, 229376\n"
    "s_mov_b32 s44, 0x3fff80\n"
    "s_lshl_b32 s45, s35, 7\n"
    "s_min_u32 s46, s36, 11\n"
    "s_mul_i32 s46, s46, 12\n"
    "s_add_u32 s46, s46, .Ltt-.Lpc0\n"
    "s_add_u32 s26, s26, s46\n"
    "s_addc_u32 s27, s27, 0\n"
    "s_mov_b32 m0, 8192\n"
    "s_cmp_eq_u32 s34, 4\n"
    "s_waitcnt lgkmcnt(0)\n"
    "s_memrealtime s[28:29]\n"
    "s_cbranch_scc0 .Lother\n"
    "global_load_lds_dwordx4 v16, s[4:5] offset:0 nt\n"
    "global_load_lds_dwordx4 v17, s[4:5] offset:1024 nt\n"
    "global_load_lds_dwordx4 v18, s[4:5] offset:2048 nt\n"
    "global_load_lds_dwordx4 v19, s[4:5] offset:3072 nt\n"
    "s_mov_b32 m0, 12288\n"
    "s_add_u32 s4, s4, 65536\n"
    "s_addc_u32 s5, s5, 0\n"
    "global_load_lds_dwordx4 v16, s[4:5] offset:0 nt\n"
    "global_load_lds_dwordx4 v17, s[4:5] offset:1024 nt\n"
    "global_load_lds_dwordx4 v18, s[4:5] offset:2048 nt\n"
    "global_load_lds_dwordx4 v19, s[4:5] offset:3072 nt\n"
    "s_mov_b32 m0, 0\n"
    "s_nop 0\n"
    "global_load_lds_dwordx4 v7, s[6:7]\n"
    "s_branch .Lload0\n"
    ".Lother:\n"
    "s_cmp_eq_u32 s34, 5\n"
    "s_waitcnt lgkmcnt(0)\n"
    "s_cbranch_scc0 .Lte\n"
    "s_setpc_b64 s[26:27]\n"
    ".Ltt:\n"
    "s_load_dword s3, s[6:7], 0\n"
    "s_branch .Lte\n"
    "s_load_dword s3, s[4:5], 0\n"
    "s_branch .Lte\n"
    "s_load_dword s3, s[4:5], s44\n"
    "s_branch .Lte\n"
    "s_load_dword s3, s[12:13], s49\n"
    "s_branch .Lte\n"
    "s_load_dword s3, s[16:17], s49\n"
    "s_branch .Lte\n"
    "s_load_dword s3, s[18:19], s50\n"
    "s_branch .Lte\n"
    "s_load_dword s3, s[20:21], s43\n"
    "s_branch .Lte\n"
    "s_load_dword s3, s[10:11], 0\n"
    "s_branch .Lte\n"
    "s_load_dword s3, s[8:9], 0\n"
    "s_branch .Lte\n"
    "s_load_dword s3, s[22:23], 0\n"
    "s_branch .Lte\n"
    "s_load_dword s3, s[24:25], s45\n"
    "s_branch .Lte\n"
    ".Lte:\n"
    "s_load_dword s42, s[14:15], 0\n"
    "s_barrier\n"
    "s_cbranch_scc1 .Lload1\n"
    "s_branch .Lcomp\n"
    ".Lload0:\n"
    "s_mul_i32 s49, s40, 28\n"
    "s_barrier\n"
    "s_sub_u32 s50, s2, s49\n"
    "s_mul_i32 s49, s40, 229376\n"
    "s_lshl_b32 s50, s50, 7\n"
    "s_add_u32 s50, s49, s50\n"
    ".Lp0:\n"
    "s_memrealtime s[30:31]\n"
    "s_waitcnt lgkmcnt(0)\n"
    "s_sub_u32 s49, s30, s28\n"
    "s_cmpk_lt_u32 s49, 12\n"
    "s_cbranch_scc1 .Lp0\n"
    "s_mov_b32 m0, 16384\n"
    "s_add_u32 s4, s4, 65536\n"
    "s_addc_u32 s5, s5, 0\n"
    "global_load_lds_dwordx4 v16, s[4:5] offset:0 nt\n"
    "global_load_lds_dwordx4 v17, s[4:5] offset:1024 nt\n"
    "global_load_lds_dwordx4 v18, s[4:5] offset:2048 nt\n"
    "global_load_lds_dwordx4 v19, s[4:5] offset:3072 nt\n"
    "s_mov_b32 m0, 20480\n"
    "s_add_u32 s4, s4, 65536\n"
    "s_addc_u32 s5, s5, 0\n"
    "global_load_lds_dwordx4 v16, s[4:5] offset:0 nt\n"
    "global_load_lds_dwordx4 v17, s[4:5] offset:1024 nt\n"
    "global_load_lds_dwordx4 v18, s[4:5] offset:2048 nt\n"
    "global_load_lds_dwordx4 v19, s[4:5] offset:3072 nt\n"
    "v_mul_u32_u24 v20, 3584, v5\n"
    "v_add_u32 v20, s50, v20\n"
    "v_add_u32 v20, v20, v6\n"
    "v_xor_b32 v21, 64, v20\n"
    "v_add_u32 v22, 55296, v20\n"
    "v_add_u32 v21, 27648, v21\n"
    "v_add_u32 v23, 55296, v21\n"
    "v_and_b32 v11, 0x60, v8\n"
    "v_xor_b32 v24, v7, v11\n"
    "v_add_u32 v24, s61, v24\n"
    "v_xor_b32 v25, 0x80, v24\n"
    "v_add_u32 v26, 63488, v24\n"
    "v_add_u32 v25, 31744, v25\n"
    "v_add_u32 v27, 63488, v25\n"
    "s_mul_i32 s50, s39, 112\n"
    "v_add_u32 v12, s50, v7\n"
    ".Lp1:\n"
    "s_memrealtime s[30:31]\n"
    "s_waitcnt lgkmcnt(0)\n"
    "s_sub_u32 s49, s30, s28\n"
    "s_cmpk_lt_u32 s49, 48\n"
    "s_cbranch_scc1 .Lp1\n"
    "s_cmpk_ge_u32 s2, 224\n"
    "s_cbranch_scc1 .Lnow1\n"
    "s_mov_b32 m0, 24576\n"
    "s_nop 0\n"
    "global_load_lds_dwordx4 v20, s[10:11] offset:0 nt\n"
    "global_load_lds_dwordx4 v21, s[10:11] offset:1024 nt\n"
    "global_load_lds_dwordx4 v22, s[10:11] offset:2048 nt\n"
    "global_load_lds_dwordx4 v23, s[10:11] offset:3072 nt\n"
    "s_mov_b32 m0, 28672\n"
    "s_add_u32 s10, s10, 114688\n"
    "s_addc_u32 s11, s11, 0\n"
    "global_load_lds_dwordx4 v20, s[10:11] offset:0 nt\n"
    "global_load_lds_dwordx4 v21, s[10:11] offset:1024 nt\n"
    "global_load_lds_dwordx4 v22, s[10:11] offset:2048 nt\n"
    "global_load_lds_dwordx4 v23, s[10:11] offset:3072 nt\n"
    ".Lnow1:\n"
    "s_mov_b32 m0, 1024\n"
    "s_cmpk_ge_u32 s2, 224\n"
    "global_load_lds_dwordx4 v7, s[8:9]\n"
    "s_mov_b32 m0, 5632\n"
    "s_mov_b64 exec, 0x7f\n"
    "global_load_lds_dwordx4 v12, s[22:23]\n"
    "s_waitcnt vmcnt(23)\n"
    "s_cbranch_scc0 .Lw1a\n"
    "s_waitcnt vmcnt(15)\n"
    ".Lw1a:\n"
    "s_memrealtime s[30:31]\n"
    "s_waitcnt lgkmcnt(0)\n"
    "s_sub_u32 s49, s30, s28\n"
    "s_sub_u32 s49, s49, 78\n"
    "s_cmp_gt_i32 s49, 0\n"
    "s_cselect_b32 s49, s49, -12\n"
    "s_add_u32 s49, s49, 12\n"
    "s_add_u32 s28, s28, s49\n"
    "s_add_u32 s49, s49, 1\n"
    "v_mov_b32 v12, s49\n"
    "s_mov_b64 exec, 1\n"
    "ds_write_b32 v14, v12 offset:5376\n"
    "s_cmpk_ge_u32 s2, 224\n"
    "s_waitcnt vmcnt(10)\n"
    "s_cbranch_scc0 .Lw1b\n"
    "s_waitcnt vmcnt(2)\n"
    ".Lw1b:\n"
    "s_mov_b64 exec, 1\n"
    "ds_add_u32 v14, v10 offset:5380\n"
    "s_mov_b64 exec, -1\n"
    "s_waitcnt vmcnt(0)\n"
    "s_mov_b64 exec, 1\n"
    "ds_add_u32 v14, v10 offset:5384\n"
    "s_mov_b64 exec, -1\n"
    ".Lp2:\n"
    "s_memrealtime s[30:31]\n"
    "s_waitcnt lgkmcnt(0)\n"
    "s_sub_u32 s49, s30, s28\n"
    "s_cmpk_lt_u32 s49, 212\n"
    "s_cbranch_scc1 .Lp2\n"
    "s_mov_b32 m0, 90112\n"
    "s_nop 0\n"
    "global_load_lds_dwordx4 v24, s[16:17] offset:0 nt\n"
    "global_load_lds_dwordx4 v25, s[16:17] offset:1024 nt\n"
    "global_load_lds_dwordx4 v26, s[16:17] offset:2048 nt\n"
    "global_load_lds_dwordx4 v27, s[16:17] offset:3072 nt\n"
    "s_mov_b32 m0, 94208\n"
    "s_add_u32 s16, s16, 131072\n"
    "s_addc_u32 s17, s17, 0\n"
    "global_load_lds_dwordx4 v24, s[16:17] offset:0 nt\n"
    "global_load_lds_dwordx4 v25, s[16:17] offset:1024 nt\n"
    "global_load_lds_dwordx4 v26, s[16:17] offset:2048 nt\n"
    "global_load_lds_dwordx4 v27, s[16:17] offset:3072 nt\n"
    ".Lp3:\n"
    "s_memrealtime s[30:31]\n"
    "s_waitcnt lgkmcnt(0)\n"
    "s_sub_u32 s49, s30, s28\n"
    "s_cmpk_lt_u32 s49, 240\n"
    "s_cbranch_scc1 .Lp3\n"
    "s_mov_b32 m0, 98304\n"
    "s_add_u32 s16, s16, 131072\n"
    "s_addc_u32 s17, s17, 0\n"
    "global_load_lds_dwordx4 v24, s[16:17] offset:0 nt\n"
    "global_load_lds_dwordx4 v25, s[16:17] offset:1024 nt\n"
    "global_load_lds_dwordx4 v26, s[16:17] offset:2048 nt\n"
    "global_load_lds_dwordx4 v27, s[16:17] offset:3072 nt\n"
    "s_mov_b32 m0, 102400\n"
    "s_add_u32 s16, s16, 131072\n"
    "s_addc_u32 s17, s17, 0\n"
    "global_load_lds_dwordx4 v24, s[16:17] offset:0 nt\n"
    "global_load_lds_dwordx4 v25, s[16:17] offset:1024 nt\n"
    "global_load_lds_dwordx4 v26, s[16:17] offset:2048 nt\n"
    "global_load_lds_dwordx4 v27, s[16:17] offset:3072 nt\n"
    ".Lp4:\n"
    "s_memrealtime s[30:31]\n"
    "s_waitcnt lgkmcnt(0)\n"
    "s_sub_u32 s49, s30, s28\n"
    "s_cmpk_lt_u32 s49, 264\n"
    "s_cbranch_scc1 .Lp4\n"
    "s_mov_b32 m0, 106496\n"
    "s_add_u32 s16, s16, 131072\n"
    "s_addc_u32 s17, s17, 0\n"
    "global_load_lds_dwordx4 v24, s[16:17] offset:0 nt\n"
    "global_load_lds_dwordx4 v25, s[16:17] offset:1024 nt\n"
    "global_load_lds_dwordx4 v26, s[16:17] offset:2048 nt\n"
    "global_load_lds_dwordx4 v27, s[16:17] offset:3072 nt\n"
    "s_mov_b32 m0, 110592\n"
    "s_add_u32 s16, s16, 131072\n"
    "s_addc_u32 s17, s17, 0\n"
    "global_load_lds_dwordx4 v24, s[16:17] offset:0 nt\n"
    "global_load_lds_dwordx4 v25, s[16:17] offset:1024 nt\n"
    "global_load_lds_dwordx4 v26, s[16:17] offset:2048 nt\n"
    "global_load_lds_dwordx4 v27, s[16:17] offset:3072 nt\n"
    ".Lp5:\n"
    "s_memrealtime s[30:31]\n"
    "s_waitcnt lgkmcnt(0)\n"
    "s_sub_u32 s49, s30, s28\n"
    "s_cmpk_lt_u32 s49, 292\n"
    "s_cbranch_scc1 .Lp5\n"
    "s_mov_b32 m0, 114688\n"
    "s_add_u32 s16, s16, 131072\n"
    "s_addc_u32 s17, s17, 0\n"
    "global_load_lds_dwordx4 v24, s[16:17] offset:0 nt\n"
    "global_load_lds_dwordx4 v25, s[16:17] offset:1024 nt\n"
    "global_load_lds_dwordx4 v26, s[16:17] offset:2048 nt\n"
    "global_load_lds_dwordx4 v27, s[16:17] offset:3072 nt\n"
    "s_mov_b32 m0, 118784\n"
    "s_add_u32 s16, s16, 131072\n"
    "s_addc_u32 s17, s17, 0\n"
    "global_load_lds_dwordx4 v24, s[16:17] offset:0 nt\n"
    "global_load_lds_dwordx4 v25, s[16:17] offset:1024 nt\n"
    "global_load_lds_dwordx4 v26, s[16:17] offset:2048 nt\n"
    "global_load_lds_dwordx4 v27, s[16:17] offset:3072 nt\n"
    "s_waitcnt vmcnt(8)\n"
    "s_mov_b64 exec, 1\n"
    "ds_add_u32 v14, v10 offset:5400\n"
    "s_mov_b64 exec, -1\n"
    "s_waitcnt vmcnt(0)\n"
    "s_mov_b64 exec, 1\n"
    "ds_add_u32 v14, v10 offset:5404\n"
    "s_mov_b64 exec, -1\n"
    "s_endpgm\n"
    ".Lload1:\n"
    "v_and_b32 v11, 0x30, v1\n"
    "v_xor_b32 v24, v7, v11\n"
    "v_add_u32 v24, s61, v24\n"
    "v_xor_b32 v25, 0x40, v24\n"
    "v_xor_b32 v26, 0x80, v24\n"
    "v_xor_b32 v27, 0xc0, v24\n"
    "v_add_u32 v25, 31744, v25\n"
    "v_add_u32 v26, 63488, v26\n"
    "v_add_u32 v27, 95232, v27\n"
    "s_lshl_b32 s50, s39, 17\n"
    "s_add_u32 s50, s50, s60\n"
    "v_and_b32 v11, 16, v8\n"
    "v_lshrrev_b32 v12, 4, v1\n"
    "v_add_u32 v11, v11, v12\n"
    "v_lshl_add_u32 v16, v11, 10, s50\n"
    "v_add_u32 v16, v16, v6\n"
    "v_xor_b32 v17, 64, v16\n"
    "v_add_u32 v18, 6144, v16\n"
    "v_add_u32 v17, 3072, v17\n"
    "v_add_u32 v19, 6144, v17\n"
    "s_mul_i32 s50, s39, 57344\n"
    "s_add_u32 s50, s50, s60\n"
    "v_lshl_add_u32 v20, v5, 10, s50\n"
    "v_add_u32 v20, v20, v6\n"
    "v_xor_b32 v21, 64, v20\n"
    "v_add_u32 v22, 14336, v20\n"
    "v_add_u32 v21, 7168, v21\n"
    "v_add_u32 v23, 14336, v21\n"
    "s_lshl_b32 s49, s39, 9\n"
    "s_add_u32 s49, s49, 16384\n"
    "v_add_u32 v34, s49, v7\n"
    "s_lshl_b32 s49, s37, 8\n"
    "s_add_u32 s49, s49, 40448\n"
    "s_mov_b32 exec_lo, 0\n"
    "v_add_u32 v34, s49, v7\n"
    "s_mov_b32 exec_lo, -1\n"
    "v_mov_b32 v11, 0\n"
    ".Lanc:\n"
    "ds_read_b32 v11, v14 offset:5376\n"
    "s_sleep 2\n"
    "v_readfirstlane_b32 s49, v11\n"
    "s_cmp_eq_u32 s49, 0\n"
    "s_cbranch_scc1 .Lanc\n"
    "s_add_u32 s28, s28, s49\n"
    "s_sub_u32 s28, s28, 1\n"
    "s_mov_b32 s30, s28\n"
    ".Lp6:\n"
    "s_memrealtime s[30:31]\n"
    "s_sleep 1\n"
    "s_sub_u32 s49, s30, s28\n"
    "s_cmpk_lt_u32 s49, 90\n"
    "s_cbranch_scc1 .Lp6\n"
    "s_mov_b32 m0, 57344\n"
    "s_nop 0\n"
    "global_load_lds_dwordx4 v24, s[12:13] offset:0 nt\n"
    "global_load_lds_dwordx4 v25, s[12:13] offset:1024 nt\n"
    "global_load_lds_dwordx4 v26, s[12:13] offset:2048 nt\n"
    "global_load_lds_dwordx4 v27, s[12:13] offset:3072 nt\n"
    "s_mov_b32 m0, 61440\n"
    "s_add_u32 s12, s12, 131072\n"
    "s_addc_u32 s13, s13, 0\n"
    "global_load_lds_dwordx4 v24, s[12:13] offset:0 nt\n"
    "global_load_lds_dwordx4 v25, s[12:13] offset:1024 nt\n"
    "global_load_lds_dwordx4 v26, s[12:13] offset:2048 nt\n"
    "global_load_lds_dwordx4 v27, s[12:13] offset:3072 nt\n"
    ".Lp7:\n"
    "s_memrealtime s[30:31]\n"
    "s_waitcnt lgkmcnt(0)\n"
    "s_sub_u32 s49, s30, s28\n"
    "s_cmpk_lt_u32 s49, 120\n"
    "s_cbranch_scc1 .Lp7\n"
    "s_mov_b32 m0, 65536\n"
    "s_add_u32 s12, s12, 131072\n"
    "s_addc_u32 s13, s13, 0\n"
    "global_load_lds_dwordx4 v24, s[12:13] offset:0 nt\n"
    "global_load_lds_dwordx4 v25, s[12:13] offset:1024 nt\n"
    "global_load_lds_dwordx4 v26, s[12:13] offset:2048 nt\n"
    "global_load_lds_dwordx4 v27, s[12:13] offset:3072 nt\n"
    "s_mov_b32 m0, 69632\n"
    "s_add_u32 s12, s12, 131072\n"
    "s_addc_u32 s13, s13, 0\n"
    "global_load_lds_dwordx4 v24, s[12:13] offset:0 nt\n"
    "global_load_lds_dwordx4 v25, s[12:13] offset:1024 nt\n"
    "global_load_lds_dwordx4 v26, s[12:13] offset:2048 nt\n"
    "global_load_lds_dwordx4 v27, s[12:13] offset:3072 nt\n"
    ".Lp8:\n"
    "s_memrealtime s[30:31]\n"
    "s_waitcnt lgkmcnt(0)\n"
    "s_sub_u32 s49, s30, s28\n"
    "s_cmpk_lt_u32 s49, 146\n"
    "s_cbranch_scc1 .Lp8\n"
    "s_mov_b32 m0, 73728\n"
    "s_add_u32 s12, s12, 131072\n"
    "s_addc_u32 s13, s13, 0\n"
    "global_load_lds_dwordx4 v24, s[12:13] offset:0 nt\n"
    "global_load_lds_dwordx4 v25, s[12:13] offset:1024 nt\n"
    "global_load_lds_dwordx4 v26, s[12:13] offset:2048 nt\n"
    "global_load_lds_dwordx4 v27, s[12:13] offset:3072 nt\n"
    "s_mov_b32 m0, 77824\n"
    "s_add_u32 s12, s12, 131072\n"
    "s_addc_u32 s13, s13, 0\n"
    "global_load_lds_dwordx4 v24, s[12:13] offset:0 nt\n"
    "global_load_lds_dwordx4 v25, s[12:13] offset:1024 nt\n"
    "global_load_lds_dwordx4 v26, s[12:13] offset:2048 nt\n"
    "global_load_lds_dwordx4 v27, s[12:13] offset:3072 nt\n"
    ".Lp9:\n"
    "s_memrealtime s[30:31]\n"
    "s_waitcnt lgkmcnt(0)\n"
    "s_sub_u32 s49, s30, s28\n"
    "s_cmpk_lt_u32 s49, 168\n"
    "s_cbranch_scc1 .Lp9\n"
    "s_mov_b32 m0, 81920\n"
    "s_add_u32 s12, s12, 131072\n"
    "s_addc_u32 s13, s13, 0\n"
    "global_load_lds_dwordx4 v24, s[12:13] offset:0 nt\n"
    "global_load_lds_dwordx4 v25, s[12:13] offset:1024 nt\n"
    "global_load_lds_dwordx4 v26, s[12:13] offset:2048 nt\n"
    "global_load_lds_dwordx4 v27, s[12:13] offset:3072 nt\n"
    "s_mov_b32 m0, 86016\n"
    "s_add_u32 s12, s12, 131072\n"
    "s_addc_u32 s13, s13, 0\n"
    "global_load_lds_dwordx4 v24, s[12:13] offset:0 nt\n"
    "global_load_lds_dwordx4 v25, s[12:13] offset:1024 nt\n"
    "global_load_lds_dwordx4 v26, s[12:13] offset:2048 nt\n"
    "global_load_lds_dwordx4 v27, s[12:13] offset:3072 nt\n"
    "s_waitcnt vmcnt(0) lgkmcnt(0)\n"
    "s_mov_b64 exec, 1\n"
    "ds_add_u32 v14, v10 offset:5388\n"
    "s_mov_b64 exec, -1\n"
    "s_add_u32 s42, s42, 1\n"
    "s_and_b32 s43, s42, 127\n"
    "s_bitset1_b32 s43, 7\n"
    "s_add_u32 s49, s42, 37\n"
    "s_and_b32 s44, s49, 127\n"
    "s_bitset1_b32 s44, 7\n"
    "s_add_u32 s49, s42, 74\n"
    "s_and_b32 s45, s49, 127\n"
    "s_bitset1_b32 s45, 7\n"
    "s_add_u32 s49, s42, 111\n"
    "s_and_b32 s46, s49, 127\n"
    "s_bitset1_b32 s46, 7\n"
    "s_mov_b32 s48, 0x100000\n"
    ".Lp10:\n"
    "s_memrealtime s[30:31]\n"
    "s_waitcnt lgkmcnt(0)\n"
    "s_sub_u32 s49, s30, s28\n"
    "s_cmpk_lt_u32 s49, 330\n"
    "s_cbranch_scc1 .Lp10\n"
    "s_mov_b32 m0, 40960\n"
    "s_nop 0\n"
    "global_load_lds_dwordx4 v16, s[18:19] offset:0 nt\n"
    "global_load_lds_dwordx4 v17, s[18:19] offset:1024 nt\n"
    "global_load_lds_dwordx4 v18, s[18:19] offset:2048 nt\n"
    "global_load_lds_dwordx4 v19, s[18:19] offset:3072 nt\n"
    "s_mov_b32 m0, 45056\n"
    "s_add_u32 s18, s18, 32768\n"
    "s_addc_u32 s19, s19, 0\n"
    "global_load_lds_dwordx4 v16, s[18:19] offset:0 nt\n"
    "global_load_lds_dwordx4 v17, s[18:19] offset:1024 nt\n"
    "global_load_lds_dwordx4 v18, s[18:19] offset:2048 nt\n"
    "global_load_lds_dwordx4 v19, s[18:19] offset:3072 nt\n"
    ".Lp11:\n"
    "s_memrealtime s[30:31]\n"
    "s_waitcnt lgkmcnt(0)\n"
    "s_sub_u32 s49, s30, s28\n"
    "s_cmpk_lt_u32 s49, 372\n"
    "s_cbranch_scc1 .Lp11\n"
    "s_mov_b32 m0, 49152\n"
    "s_add_u32 s18, s18, 32768\n"
    "s_addc_u32 s19, s19, 0\n"
    "global_load_lds_dwordx4 v16, s[18:19] offset:0 nt\n"
    "global_load_lds_dwordx4 v17, s[18:19] offset:1024 nt\n"
    "global_load_lds_dwordx4 v18, s[18:19] offset:2048 nt\n"
    "global_load_lds_dwordx4 v19, s[18:19] offset:3072 nt\n"
    "s_mov_b32 m0, 53248\n"
    "s_add_u32 s18, s18, 32768\n"
    "s_addc_u32 s19, s19, 0\n"
    "global_load_lds_dwordx4 v16, s[18:19] offset:0 nt\n"
    "global_load_lds_dwordx4 v17, s[18:19] offset:1024 nt\n"
    "global_load_lds_dwordx4 v18, s[18:19] offset:2048 nt\n"
    "global_load_lds_dwordx4 v19, s[18:19] offset:3072 nt\n"
    ".Lp12:\n"
    "s_memrealtime s[30:31]\n"
    "s_waitcnt lgkmcnt(0)\n"
    "s_sub_u32 s49, s30, s28\n"
    "s_cmpk_lt_u32 s49, 398\n"
    "s_cbranch_scc1 .Lp12\n"
    "s_mov_b32 exec_hi, 0xffff\n"
    "global_load_dwordx4 v[28:31], v34, s[14:15] sc1\n"
    "s_mov_b64 exec, -1\n"
    "s_mov_b32 m0, 32768\n"
    "s_nop 0\n"
    "global_load_lds_dwordx4 v20, s[20:21] offset:0 nt\n"
    "global_load_lds_dwordx4 v21, s[20:21] offset:1024 nt\n"
    "global_load_lds_dwordx4 v22, s[20:21] offset:2048 nt\n"
    "global_load_lds_dwordx4 v23, s[20:21] offset:3072 nt\n"
    "s_mov_b32 m0, 36864\n"
    "s_add_u32 s20, s20, 32768\n"
    "s_addc_u32 s21, s21, 0\n"
    "global_load_lds_dwordx4 v20, s[20:21] offset:0 nt\n"
    "global_load_lds_dwordx4 v21, s[20:21] offset:1024 nt\n"
    "global_load_lds_dwordx4 v22, s[20:21] offset:2048 nt\n"
    "s_waitcnt vmcnt(8)\n"
    "s_mov_b64 exec, 1\n"
    "ds_add_u32 v14, v10 offset:5408\n"
    "s_mov_b64 exec, -1\n"
    "s_mov_b32 exec_hi, 0xffff\n"
    "s_waitcnt vmcnt(7)\n"
    ".Lm_chk:\n"
    "v_xor_b32 v28, s43, v28\n"
    "v_xor_b32 v29, s44, v29\n"
    "v_xor_b32 v30, s45, v30\n"
    "v_xor_b32 v31, s46, v31\n"
    "v_or_b32 v5, v28, v29\n"
    "v_or3_b32 v5, v5, v30, v31\n"
    "v_cmp_ne_u32_sdwa vcc, v5, v14 src0_sel:BYTE_0 src1_sel:DWORD\n"
    "s_cbranch_vccnz .Lm_retry\n"
    ".Lm_ok:\n"
    "s_mov_b32 exec_lo, -1\n"
    "s_mov_b32 exec_hi, 0\n"
    "v_max_f32_dpp v32, v28, v28 quad_perm:[1,0,3,2] row_mask:0xf bank_mask:0xf bound_ctrl:1\n"
    "s_nop 1\n"
    "v_max_f32_dpp v32, v32, v32 quad_perm:[2,3,0,1] row_mask:0xf bank_mask:0xf bound_ctrl:1\n"
    "s_nop 1\n"
    "v_max_f32_dpp v32, v32, v32 row_ror:4 row_mask:0xf bank_mask:0xf bound_ctrl:1\n"
    "s_nop 1\n"
    "v_max_f32_dpp v32, v32, v32 row_ror:8 row_mask:0xf bank_mask:0xf bound_ctrl:1\n"
    "v_mov_b32 v5, v32\n"
    "s_nop 1\n"
    "v_permlane16_swap_b32 v32, v5\n"
    "v_max_f32 v32, v32, v5\n"
    "v_sub_f32 v32, v28, v32\n"
    "v_mul_f32 v32, 0x3fb8aa3b, v32\n"
    "v_exp_f32 v32, v32\n"
    "s_nop 0\n"
    "v_mul_f32 v33, v32, v29\n"
    "s_nop 1\n"
    "v_add_f32_dpp v33, v33, v33 quad_perm:[1,0,3,2] row_mask:0xf bank_mask:0xf bound_ctrl:1\n"
    "s_nop 1\n"
    "v_add_f32_dpp v33, v33, v33 quad_perm:[2,3,0,1] row_mask:0xf bank_mask:0xf bound_ctrl:1\n"
    "s_nop 1\n"
    "v_add_f32_dpp v33, v33, v33 row_ror:4 row_mask:0xf bank_mask:0xf bound_ctrl:1\n"
    "s_nop 1\n"
    "v_add_f32_dpp v33, v33, v33 row_ror:8 row_mask:0xf bank_mask:0xf bound_ctrl:1\n"
    "v_mov_b32 v5, v33\n"
    "s_nop 1\n"
    "v_permlane16_swap_b32 v33, v5\n"
    "v_add_f32 v33, v33, v5\n"
    "v_rcp_f32 v33, v33\n"
    "v_lshlrev_b32 v5, 2, v1\n"
    "v_mul_f32 v32, v32, v33\n"
    "ds_write_b32 v5, v32 offset:5888\n"
    "s_waitcnt vmcnt(0) lgkmcnt(0)\n"
    "s_endpgm\n"
    ".Lcomp:\n"
    "v_and_b32 v2, 15, v0\n"
    "v_bfe_u32 v3, v0, 4, 2\n"
    "v_bfe_u32 v4, v0, 2, 2\n"
    "v_and_b32 v8, 3, v0\n"
    "s_lshl_b32 s49, s35, 15\n"
    "s_lshl_b32 s50, s36, 8\n"
    "s_add_u32 s49, s49, s50\n"
    "s_lshl_b32 s50, s34, 6\n"
    "s_add_u32 s49, s49, s50\n"
    "s_add_u32 s49, s49, 294912\n"
    "v_lshl_add_u32 v50, v3, 13, s49\n"
    "v_lshl_add_u32 v50, v2, 2, v50\n"
    "v_mov_b32 v5, -1\n"
    "global_store_dword v50, v5, s[14:15] sc1\n"
    "s_lshl_b32 s49, s37, 10\n"
    "s_lshl_b32 s60, s34, 8\n"
    "s_add_u32 s49, s49, s60\n"
    "s_add_u32 s49, s49, 8192\n"
    "v_lshl_add_u32 v13, v2, 3, s49\n"
    "s_lshl_b32 s49, s41, 13\n"
    "s_lshl_b32 s60, s36, 8\n"
    "s_add_u32 s49, s49, s60\n"
    "s_add_u32 s49, s49, s50\n"
    "s_add_u32 s49, s49, 32768\n"
    "v_lshl_add_u32 v39, v2, 2, s49\n"
    "s_lshl_b32 s49, s2, 8\n"
    "s_add_u32 s49, s49, s50\n"
    "s_add_u32 s49, s49, 98304\n"
    "v_lshl_add_u32 v40, v2, 2, s49\n"
    "s_mov_b64 exec, 0xffff\n"
    "global_store_dword v39, v14, s[14:15] sc1\n"
    "s_cmpk_ge_u32 s2, 224\n"
    "s_cbranch_scc1 .Lnoph\n"
    "global_store_dword v40, v14, s[14:15] sc1\n"
    ".Lnoph:\n"
    "s_and_b32 s50, s2, 0xc7\n"
    "s_cbranch_scc1 .Lnoz\n"
    "global_store_dwordx2 v13, v[14:15], s[14:15] sc1\n"
    ".Lnoz:\n"
    "s_cmp_eq_u32 s34, 1\n"
    "s_cbranch_scc0 .Lnopm\n"
    "s_lshl_b32 s49, s41, 11\n"
    "s_lshl_b32 s50, s36, 6\n"
    "s_add_u32 s49, s49, s50\n"
    "s_add_u32 s49, s49, 16384\n"
    "v_lshl_add_u32 v5, v2, 2, s49\n"
    "global_store_dword v5, v14, s[14:15] sc1\n"
    ".Lnopm:\n"
    "s_mov_b64 exec, -1\n"
    "s_lshl_b32 s49, s35, 11\n"
    "s_lshl_b32 s50, s36, 4\n"
    "s_add_u32 s49, s49, s50\n"
    "s_add_u32 s49, s49, 16384\n"
    "v_lshl_add_u32 v51, v4, 9, s49\n"
    "v_bfe_u32 v5, v0, 3, 2\n"
    "v_xor_b32 v5, s34, v5\n"
    "v_lshlrev_b32 v35, 10, v3\n"
    "v_lshl_or_b32 v35, v4, 7, v35\n"
    "v_lshl_or_b32 v35, v5, 5, v35\n"
    "v_lshl_or_b32 v35, v8, 3, v35\n"
    "v_lshlrev_b32 v38, 4, v3\n"
    "s_lshl_b32 s49, s38, 6\n"
    "v_lshl_add_u32 v36, v3, 8, s49\n"
    "s_lshl_b32 s49, s40, 7\n"
    "v_add_u32 v37, s49, v38\n"
    "v_lshlrev_b32 v61, 4, v1\n"
    "s_mov_b32 s47, 0xffffff00\n"
    "s_mov_b32 s62, 0x3fb8aa3b\n"
    "s_mov_b32 s63, 0x3fb8aa3b\n"
    "s_mov_b32 s64, 0x3db504f3\n"
    "s_mov_b32 s65, 0x3db504f3\n"
    "s_mov_b32 s52, 0x11111111\n"
    "s_mov_b32 s53, 0x11111111\n"
    "s_mov_b32 s54, 0xff\n"
    "s_mov_b32 s55, 0xff\n"
    "s_mov_b32 s56, 0\n"
    "s_mov_b32 s57, 0x40f04000\n"
    "s_mov_b32 s58, 0\n"
    "s_mov_b32 s59, 0x41040000\n"
    "v_lshrrev_b32 v5, 5, v1\n"
    "v_and_b32 v6, 31, v0\n"
    "s_lshl_b32 s49, s35, 13\n"
    "s_lshl_b32 s50, s34, 9\n"
    "s_add_u32 s49, s49, s50\n"
    "s_add_u32 s49, s49, 32768\n"
    "v_lshl_add_u32 v41, v5, 12, s49\n"
    "v_lshl_add_u32 v41, v6, 4, v41\n"
    "s_mul_i32 s49, s34, 320\n"
    "s_add_u32 s49, s49, 2048\n"
    "v_lshl_add_u32 v44, v6, 3, s49\n"
    "v_lshl_add_u32 v44, v5, 2, v44\n"
    "v_and_b32 v6, 7, v0\n"
    "s_lshl_b32 s49, s34, 4\n"
    "v_lshl_add_u32 v62, v6, 1, s49\n"
    "v_add_u32 v55, v62, v5\n"
    "v_lshlrev_b32 v55, 1, v55\n"
    "v_cmp_le_u32 vcc, 56, v62\n"
    "v_subrev_u32 v7, 8, v62\n"
    "s_mul_i32 s49, s39, 56\n"
    "v_cndmask_b32 v7, v62, v7, vcc\n"
    "v_add_lshl_u32 v43, v7, v5, 1\n"
    "v_add_u32 v7, s49, v7\n"
    "v_bfe_u32 v5, v0, 3, 3\n"
    "v_mul_u32_u24 v42, 1792, v5\n"
    "v_add_lshl_u32 v42, v42, v7, 2\n"
    "v_add_u32 v42, 98304, v42\n"
    "s_lshl_b32 s49, s39, 13\n"
    "s_lshl_b32 s50, s34, 11\n"
    "s_add_u32 s49, s49, s50\n"
    "s_add_u32 s49, s49, 294912\n"
    "v_add_u32 v52, s49, v61\n"
    "s_and_b32 s49, s34, 1\n"
    "s_lshl_b32 s49, s49, 7\n"
    "v_xor_b32 v53, v61, v38\n"
    "v_xor_b32 v53, s49, v53\n"
    "v_add_u32 v53, s50, v53\n"
    "v_xor_b32 v54, 64, v53\n"
    "v_xor_b32 v7, v3, v2\n"
    "v_lshlrev_b32 v16, 8, v2\n"
    "v_lshl_add_u32 v16, v7, 4, v16\n"
    "v_xor_b32 v17, 64, v16\n"
    "v_xor_b32 v18, 0x80, v16\n"
    "v_xor_b32 v19, 0xc0, v16\n"
    "s_lshl_b32 s49, s34, 13\n"
    "s_add_u32 s49, s49, 57344\n"
    "v_add_u32 v64, s49, v16\n"
    "v_add_u32 v65, s49, v17\n"
    "v_add_u32 v66, s49, v18\n"
    "v_add_u32 v67, s49, v19\n"
    "v_bfe_u32 v5, v0, 2, 4\n"
    "v_bfe_u32 v7, v0, 2, 3\n"
    "s_lshl_b32 s49, s34, 1\n"
    "v_xor_b32 v7, s49, v7\n"
    "v_lshlrev_b32 v68, 8, v5\n"
    "v_lshl_or_b32 v68, v7, 5, v68\n"
    "v_lshl_or_b32 v68, v8, 3, v68\n"
    "v_add_u32 v68, 90112, v68\n"
    "v_xor_b32 v69, 32, v68\n"
    "v_mul_u32_u24 v45, 320, v4\n"
    "v_add_u32 v45, v45, v38\n"
    "v_lshlrev_b32 v47, 4, v4\n"
    "s_lshl_b32 s49, s34, 2\n"
    "v_add_u32 v46, s49, v47\n"
    "v_lshlrev_b32 v48, 4, v5\n"
    "s_lshl_b32 s49, s34, 8\n"
    "v_add_u32 v49, s49, v48\n"
    "s_lshl_b32 s49, s37, 14\n"
    "s_lshl_b32 s50, s34, 7\n"
    "s_add_u32 s49, s49, s50\n"
    "s_add_u32 s49, s49, 163840\n"
    "v_lshl_add_u32 v56, v2, 3, s49\n"
    "s_lshl_b32 s49, s37, 7\n"
    "s_lshl_b32 s50, s34, 5\n"
    "s_add_u32 s50, s49, s50\n"
    "v_lshl_add_u32 v57, v2, 1, s50\n"
    "s_waitcnt lgkmcnt(0)\n"
    "s_add_u32 s42, s42, 1\n"
    "s_and_b32 s43, s42, 127\n"
    "s_bitset1_b32 s43, 7\n"
    "s_add_u32 s49, s42, 37\n"
    "s_and_b32 s44, s49, 127\n"
    "s_bitset1_b32 s44, 7\n"
    "s_add_u32 s49, s42, 74\n"
    "s_and_b32 s45, s49, 127\n"
    "s_bitset1_b32 s45, 7\n"
    "s_add_u32 s49, s42, 111\n"
    "s_and_b32 s46, s49, 127\n"
    "s_bitset1_b32 s46, 7\n"
    "s_mov_b32 s48, 0x100000\n"
    "s_movk_i32 s49, 127\n"
    "v_mov_b32 v5, 128\n"
    "v_mul_u32_u24 v58, 37, v8\n"
    "v_add_u32 v58, s42, v58\n"
    "v_and_or_b32 v58, v58, s49, v5\n"
    "v_and_b32 v7, 1, v0\n"
    "v_mul_u32_u24 v7, 74, v7\n"
    "v_add_u32 v59, s42, v7\n"
    "v_add_u32 v60, 37, v59\n"
    "v_and_or_b32 v59, v59, s49, v5\n"
    "v_and_or_b32 v60, v60, s49, v5\n"
    ".Lw13:\n"
    "ds_read_b32 v11, v14 offset:5380\n"
    "ds_read_b32 v12, v14 offset:5376\n"
    "s_waitcnt lgkmcnt(1)\n"
    "v_cmp_ne_u32 vcc, 1, v11\n"
    "s_cbranch_vccnz .Lw13\n"
    "ds_read_b128 v[70:73], v36 offset:0\n"
    "ds_read_b64_tr_b16 v[86:87], v35 offset:8192\n"
    "ds_read_b64_tr_b16 v[88:89], v35 offset:8704\n"
    "ds_read_b128 v[74:77], v36 offset:16\n"
    "ds_read_b64_tr_b16 v[90:91], v35 offset:12288\n"
    "ds_read_b64_tr_b16 v[92:93], v35 offset:12800\n"
    "ds_read_b128 v[78:81], v36 offset:32\n"
    "ds_read_b64_tr_b16 v[94:95], v35 offset:16384\n"
    "ds_read_b64_tr_b16 v[96:97], v35 offset:16896\n"
    "ds_read_b128 v[82:85], v36 offset:48\n"
    "ds_read_b64_tr_b16 v[98:99], v35 offset:20480\n"
    "ds_read_b64_tr_b16 v[100:101], v35 offset:20992\n"
    "s_waitcnt lgkmcnt(9)\n"
    "v_mfma_f32_16x16x32_bf16 v[134:137], v[70:73], v[86:89], 0\n"
    "s_waitcnt lgkmcnt(6)\n"
    "v_mfma_f32_16x16x32_bf16 v[134:137], v[74:77], v[90:93], v[134:137]\n"
    "s_waitcnt lgkmcnt(3)\n"
    "v_mfma_f32_16x16x32_bf16 v[134:137], v[78:81], v[94:97], v[134:137]\n"
    "s_waitcnt lgkmcnt(0)\n"
    "v_mfma_f32_16x16x32_bf16 v[134:137], v[82:85], v[98:101], v[134:137]\n"
    "v_readfirstlane_b32 s49, v12\n"
    "s_add_u32 s28, s28, s49\n"
    "s_sub_u32 s28, s28, 1\n"
    "s_mov_b64 exec, 0xffff\n"
    "s_cmpk_lt_u32 s2, 224\n"
    "s_waitcnt vmcnt(0)\n"
    "s_nop 1\n"
    "v_and_or_b32 v5, v134, s47, v58\n"
    "global_store_dword v39, v5, s[14:15] sc1\n"
    "s_mov_b64 exec, -1\n"
    ".Lw14:\n"
    "ds_read_b32 v11, v14 offset:5384\n"
    "s_waitcnt lgkmcnt(0)\n"
    "v_cmp_ne_u32 vcc, 1, v11\n"
    "s_cbranch_vccnz .Lw14\n"
    "s_cbranch_scc0 .Lnoh\n"
    "ds_read_b128 v[70:73], v37 offset:0\n"
    "ds_read_b64_tr_b16 v[86:87], v35 offset:24576\n"
    "ds_read_b64_tr_b16 v[88:89], v35 offset:25088\n"
    "ds_read_b128 v[74:77], v37 offset:64\n"
    "ds_read_b64_tr_b16 v[90:91], v35 offset:28672\n"
    "ds_read_b64_tr_b16 v[92:93], v35 offset:29184\n"
    "s_waitcnt lgkmcnt(3)\n"
    "v_mfma_f32_16x16x32_bf16 v[138:141], v[70:73], v[86:89], 0\n"
    "s_waitcnt lgkmcnt(0)\n"
    "v_mfma_f32_16x16x32_bf16 v[138:141], v[74:77], v[90:93], v[138:141]\n"
    "s_mov_b64 exec, 0xffff\n"
    "s_nop 6\n"
    "v_and_or_b32 v5, v138, s47, v58\n"
    "global_store_dword v40, v5, s[14:15] sc1\n"
    "s_mov_b64 exec, -1\n"
    ".Lnoh:\n"
    "s_lshl_b32 s49, s34, 5\n"
    "s_or_b32 s49, s49, s36\n"
    "s_cmp_eq_u32 s49, 28\n"
    "s_cbranch_scc0 .Lnouns\n"
    "s_mov_b64 exec, 0xffff\n"
    "s_lshl_b32 s49, s35, 7\n"
    "v_lshl_add_u32 v5, v2, 3, s49\n"
    "global_store_dwordx2 v5, v[14:15], s[24:25] sc1\n"
    "s_cmp_eq_u32 s35, 0\n"
    "s_cbranch_scc0 .Lnouns1\n"
    "s_mov_b64 exec, 1\n"
    "global_store_dword v14, v14, s[14:15] offset:4 sc1\n"
    ".Lnouns1:\n"
    "s_mov_b64 exec, -1\n"
    ".Lnouns:\n"
    "ds_read_b128 v[70:73], v61 offset:0\n"
    "ds_read_b128 v[74:77], v61 offset:1024\n"
    "ds_read_u16 v63, v43 offset:5632\n"
    "v_mov_b32 v146, 0\n"
    "v_mov_b32 v7, 0x3f803f80\n"
    "s_waitcnt lgkmcnt(0)\n"
    "v_lshlrev_b32 v78, 16, v70\n"
    "v_lshlrev_b32 v82, 16, v74\n"
    "v_and_b32 v70, 0xffff0000, v70\n"
    "v_and_b32 v74, 0xffff0000, v74\n"
    "v_mul_f32 v78, v78, v82\n"
    "v_mul_f32 v70, v70, v74\n"
    "v_cvt_pk_bf16_f32 v70, v78, v70\n"
    "v_lshlrev_b32 v79, 16, v71\n"
    "v_lshlrev_b32 v83, 16, v75\n"
    "v_and_b32 v71, 0xffff0000, v71\n"
    "v_and_b32 v75, 0xffff0000, v75\n"
    "v_mul_f32 v79, v79, v83\n"
    "v_mul_f32 v71, v71, v75\n"
    "v_cvt_pk_bf16_f32 v71, v79, v71\n"
    "v_lshlrev_b32 v80, 16, v72\n"
    "v_lshlrev_b32 v84, 16, v76\n"
    "v_and_b32 v72, 0xffff0000, v72\n"
    "v_and_b32 v76, 0xffff0000, v76\n"
    "v_mul_f32 v80, v80, v84\n"
    "v_mul_f32 v72, v72, v76\n"
    "v_cvt_pk_bf16_f32 v72, v80, v72\n"
    "v_lshlrev_b32 v81, 16, v73\n"
    "v_lshlrev_b32 v85, 16, v77\n"
    "v_and_b32 v73, 0xffff0000, v73\n"
    "v_and_b32 v77, 0xffff0000, v77\n"
    "v_mul_f32 v81, v81, v85\n"
    "v_mul_f32 v73, v73, v77\n"
    "v_cvt_pk_bf16_f32 v73, v81, v73\n"
    "v_dot2c_f32_bf16 v146, v70, v7\n"
    "v_dot2c_f32_bf16 v146, v71, v7\n"
    "v_dot2c_f32_bf16 v146, v72, v7\n"
    "v_dot2c_f32_bf16 v146, v73, v7\n"
    "s_nop 0\n"
    "s_nop 1\n"
    "v_add_f32_dpp v146, v146, v146 quad_perm:[1,0,3,2] row_mask:0xf bank_mask:0xf bound_ctrl:1\n"
    "s_nop 1\n"
    "v_add_f32_dpp v146, v146, v146 quad_perm:[2,3,0,1] row_mask:0xf bank_mask:0xf bound_ctrl:1\n"
    "s_nop 1\n"
    "v_add_f32_dpp v146, v146, v146 row_ror:4 row_mask:0xf bank_mask:0xf bound_ctrl:1\n"
    "s_nop 1\n"
    "v_add_f32_dpp v146, v146, v146 row_ror:8 row_mask:0xf bank_mask:0xf bound_ctrl:1\n"
    "v_mov_b32 v5, v146\n"
    "s_nop 1\n"
    "v_permlane16_swap_b32 v146, v5\n"
    "v_add_f32 v146, v146, v5\n"
    "v_mov_b32 v5, v146\n"
    "s_nop 1\n"
    "v_permlane32_swap_b32 v146, v5\n"
    "v_add_f32 v146, v146, v5\n"
    "v_cvt_pk_bf16_f32 v146, 0, v146\n"
    "v_mul_f32 v146, 0xbfb8aa3b, v146\n"
    "v_exp_f32 v146, v146\n"
    "s_nop 0\n"
    "v_add_f32 v146, 1.0, v146\n"
    "v_rcp_f32 v146, v146\n"
    "s_nop 0\n"
    "v_cvt_pk_bf16_f32 v146, 0, v146\n"
    "v_sub_f32 v147, 1.0, v146\n"
    "v_cvt_pk_bf16_f32 v147, 0, v147\n"
    "v_cvt_f64_f32 v[148:149], v146\n"
    ".Lp15:\n"
    "s_memrealtime s[30:31]\n"
    "s_waitcnt lgkmcnt(0)\n"
    "s_sub_u32 s49, s30, s28\n"
    "s_cmpk_lt_u32 s49, 200\n"
    "s_cbranch_scc1 .Lp15\n"
    "global_load_dwordx4 v[150:153], v41, s[14:15] sc1\n"
    "global_load_dwordx4 v[154:157], v41, s[14:15] offset:2048 sc1\n"
    "s_waitcnt vmcnt(0)\n"
    ".Lq_chk:\n"
    "v_xor_b32 v150, s43, v150\n"
    "v_xor_b32 v151, s44, v151\n"
    "v_xor_b32 v152, s45, v152\n"
    "v_xor_b32 v153, s46, v153\n"
    "v_xor_b32 v154, s43, v154\n"
    "v_xor_b32 v155, s44, v155\n"
    "v_xor_b32 v156, s45, v156\n"
    "v_xor_b32 v157, s46, v157\n"
    "v_or_b32 v5, v150, v151\n"
    "v_or3_b32 v5, v5, v152, v153\n"
    "v_or3_b32 v5, v5, v154, v155\n"
    "v_or3_b32 v5, v5, v156, v157\n"
    "v_cmp_ne_u32_sdwa vcc, v5, v14 src0_sel:BYTE_0 src1_sel:DWORD\n"
    "s_cbranch_vccnz .Lq_retry\n"
    ".Lq_ok:\n"
    "s_mov_b64 exec, -1\n"
    "v_pk_add_f32 v[150:151], v[150:151], v[154:155]\n"
    "v_pk_add_f32 v[152:153], v[152:153], v[156:157]\n"
    "s_nop 1\n"
    "v_permlane32_swap_b32 v150, v152\n"
    "v_permlane32_swap_b32 v151, v153\n"
    "v_pk_add_f32 v[150:151], v[150:151], v[152:153]\n"
    "v_cvt_pk_bf16_f32 v5, v150, v151\n"
    "ds_write_b32 v44, v5\n"
    "s_mov_b64 exec, 1\n"
    "ds_add_u32 v14, v10 offset:5388\n"
    "s_mov_b64 exec, -1\n"
    ".Lw16:\n"
    "ds_read_b32 v11, v14 offset:5388\n"
    "ds_read_b128 v[70:73], v45 offset:2048\n"
    "ds_read_b128 v[86:89], v64\n"
    "ds_read_b128 v[102:105], v64 offset:4096\n"
    "s_waitcnt lgkmcnt(3)\n"
    "v_cmp_ne_u32 vcc, 5, v11\n"
    "s_cbranch_vccnz .Lw16\n"
    "ds_read_b128 v[74:77], v45 offset:2112\n"
    "ds_read_b128 v[90:93], v65\n"
    "ds_read_b128 v[106:109], v65 offset:4096\n"
    "ds_read_b128 v[78:81], v45 offset:2176\n"
    "ds_read_b128 v[94:97], v66\n"
    "ds_read_b128 v[110:113], v66 offset:4096\n"
    "ds_read_b128 v[82:85], v45 offset:2240\n"
    "ds_read_b128 v[98:101], v67\n"
    "ds_read_b128 v[114:117], v67 offset:4096\n"
    "s_waitcnt lgkmcnt(10)\n"
    "v_mfma_f32_16x16x32_bf16 v[134:137], v[86:89], v[70:73], 0\n"
    "s_waitcnt lgkmcnt(9)\n"
    "v_mfma_f32_16x16x32_bf16 v[138:141], v[102:105], v[70:73], 0\n"
    "s_waitcnt lgkmcnt(7)\n"
    "v_mfma_f32_16x16x32_bf16 v[134:137], v[90:93], v[74:77], v[134:137]\n"
    "s_waitcnt lgkmcnt(6)\n"
    "v_mfma_f32_16x16x32_bf16 v[138:141], v[106:109], v[74:77], v[138:141]\n"
    "s_waitcnt lgkmcnt(4)\n"
    "v_mfma_f32_16x16x32_bf16 v[134:137], v[94:97], v[78:81], v[134:137]\n"
    "s_waitcnt lgkmcnt(3)\n"
    "v_mfma_f32_16x16x32_bf16 v[138:141], v[110:113], v[78:81], v[138:141]\n"
    "s_waitcnt lgkmcnt(1)\n"
    "v_mfma_f32_16x16x32_bf16 v[134:137], v[98:101], v[82:85], v[134:137]\n"
    "s_waitcnt lgkmcnt(0)\n"
    "v_mfma_f32_16x16x32_bf16 v[138:141], v[114:117], v[82:85], v[138:141]\n"
    "s_nop 5\n"
    "v_max3_f32 v160, v134, v135, v136\n"
    "s_nop 0\n"
    "v_max3_f32 v160, v160, v137, v138\n"
    "v_max3_f32 v160, v160, v139, v140\n"
    "v_max_f32 v160, v160, v141\n"
    "v_mov_b32 v5, v160\n"
    "s_nop 1\n"
    "v_permlane16_swap_b32 v160, v5\n"
    "v_max_f32 v160, v160, v5\n"
    "v_mov_b32 v5, v160\n"
    "s_nop 1\n"
    "v_permlane32_swap_b32 v160, v5\n"
    "v_max_f32 v160, v160, v5\n"
    "s_mov_b64 exec, 0x1111\n"
    "ds_write_b32 v46, v160 offset:4608\n"
    "s_mov_b64 exec, 1\n"
    "ds_add_u32 v14, v10 offset:5392\n"
    "s_mov_b64 exec, -1\n"
    "v_cvt_pk_bf16_f32 v134, 0, v134\n"
    "v_cvt_pk_bf16_f32 v135, 0, v135\n"
    "v_cvt_pk_bf16_f32 v136, 0, v136\n"
    "v_cvt_pk_bf16_f32 v137, 0, v137\n"
    "v_cvt_pk_bf16_f32 v138, 0, v138\n"
    "v_cvt_pk_bf16_f32 v139, 0, v139\n"
    "v_cvt_pk_bf16_f32 v140, 0, v140\n"
    "v_cvt_pk_bf16_f32 v141, 0, v141\n"
    "v_pk_mul_f32 v[134:135], v[134:135], s[64:65]\n"
    "v_pk_mul_f32 v[136:137], v[136:137], s[64:65]\n"
    "v_pk_mul_f32 v[138:139], v[138:139], s[64:65]\n"
    "v_pk_mul_f32 v[140:141], v[140:141], s[64:65]\n"
    "v_cvt_pk_bf16_f32 v134, 0, v134\n"
    "v_cvt_pk_bf16_f32 v135, 0, v135\n"
    "v_cvt_pk_bf16_f32 v136, 0, v136\n"
    "v_cvt_pk_bf16_f32 v137, 0, v137\n"
    "v_cvt_pk_bf16_f32 v138, 0, v138\n"
    "v_cvt_pk_bf16_f32 v139, 0, v139\n"
    "v_cvt_pk_bf16_f32 v140, 0, v140\n"
    "v_cvt_pk_bf16_f32 v141, 0, v141\n"
    ".Lw17:\n"
    "ds_read_b32 v11, v14 offset:5392\n"
    "ds_read_b128 v[70:73], v47 offset:4608\n"
    "s_waitcnt lgkmcnt(1)\n"
    "v_cmp_ne_u32 vcc, 4, v11\n"
    "s_cbranch_vccnz .Lw17\n"
    "s_waitcnt lgkmcnt(0)\n"
    "v_max3_f32 v160, v70, v71, v72\n"
    "v_max_f32 v160, v160, v73\n"
    "v_cvt_pk_bf16_f32 v160, 0, v160\n"
    "v_mul_f32 v160, s64, v160\n"
    "v_cvt_pk_bf16_f32 v160, 0, v160\n"
    "v_pk_add_f32 v[134:135], v[134:135], v[160:161] op_sel_hi:[1,0] neg_lo:[0,1] neg_hi:[0,1]\n"
    "v_pk_add_f32 v[136:137], v[136:137], v[160:161] op_sel_hi:[1,0] neg_lo:[0,1] neg_hi:[0,1]\n"
    "v_pk_add_f32 v[138:139], v[138:139], v[160:161] op_sel_hi:[1,0] neg_lo:[0,1] neg_hi:[0,1]\n"
    "v_pk_add_f32 v[140:141], v[140:141], v[160:161] op_sel_hi:[1,0] neg_lo:[0,1] neg_hi:[0,1]\n"
    "v_pk_mul_f32 v[134:135], v[134:135], s[62:63]\n"
    "v_pk_mul_f32 v[136:137], v[136:137], s[62:63]\n"
    "v_pk_mul_f32 v[138:139], v[138:139], s[62:63]\n"
    "v_pk_mul_f32 v[140:141], v[140:141], s[62:63]\n"
    "v_exp_f32 v134, v134\n"
    "v_exp_f32 v135, v135\n"
    "v_exp_f32 v136, v136\n"
    "v_exp_f32 v137, v137\n"
    "v_exp_f32 v138, v138\n"
    "v_exp_f32 v139, v139\n"
    "v_exp_f32 v140, v140\n"
    "v_exp_f32 v141, v141\n"
    "v_cvt_pk_bf16_f32 v86, v134, v135\n"
    "v_cvt_pk_bf16_f32 v87, v136, v137\n"
    "v_cvt_pk_bf16_f32 v88, v138, v139\n"
    "v_cvt_pk_bf16_f32 v89, v140, v141\n"
    "s_mov_b64 exec, s[52:53]\n"
    "ds_write_b128 v49, v[86:89] offset:3584\n"
    "s_mov_b64 exec, 1\n"
    "ds_add_u32 v14, v10 offset:5396\n"
    "s_mov_b64 exec, -1\n"
    ".Lw18:\n"
    "ds_read_b32 v11, v14 offset:5396\n"
    "ds_read_b128 v[70:73], v48 offset:3584\n"
    "ds_read_b128 v[74:77], v48 offset:3840\n"
    "ds_read_b128 v[78:81], v48 offset:4096\n"
    "ds_read_b128 v[82:85], v48 offset:4352\n"
    "s_waitcnt lgkmcnt(4)\n"
    "v_cmp_ne_u32 vcc, 4, v11\n"
    "s_cbranch_vccnz .Lw18\n"
    "s_cmp_eq_u32 s34, 3\n"
    "s_cbranch_scc0 .Lnoml\n"
    "v_mov_b32 v118, 0x3f803f80\n"
    "v_mov_b32 v119, 0x3f803f80\n"
    "v_mov_b32 v120, 0x3f803f80\n"
    "v_mov_b32 v121, 0x3f803f80\n"
    "v_or_b32 v122, s43, v160\n"
    "v_or_b32 v124, s45, v160\n"
    "s_waitcnt lgkmcnt(0)\n"
    "v_mfma_f32_16x16x32_bf16 v[142:145], v[118:121], v[70:73], 0\n"
    "v_mfma_f32_16x16x32_bf16 v[142:145], v[118:121], v[74:77], v[142:145]\n"
    "v_mfma_f32_16x16x32_bf16 v[142:145], v[118:121], v[78:81], v[142:145]\n"
    "v_mfma_f32_16x16x32_bf16 v[142:145], v[118:121], v[82:85], v[142:145]\n"
    "s_mov_b64 exec, 0x1111\n"
    "s_nop 6\n"
    "v_and_b32 v123, s47, v142\n"
    "v_or_b32 v125, s46, v123\n"
    "v_or_b32 v123, s44, v123\n"
    "global_store_dwordx4 v51, v[122:125], s[14:15] sc1\n"
    "s_mov_b64 exec, -1\n"
    ".Lnoml:\n"
    "global_load_dwordx2 v[158:159], v42, s[14:15] sc1\n"
    ".Lw19:\n"
    "ds_read_b32 v11, v14 offset:5400\n"
    "s_waitcnt lgkmcnt(0)\n"
    "v_cmp_ne_u32 vcc, 1, v11\n"
    "s_cbranch_vccnz .Lw19\n"
    "ds_read_b64_tr_b16 v[86:87], v68 offset:0\n"
    "ds_read_b64_tr_b16 v[88:89], v68 offset:4096\n"
    "ds_read_b64_tr_b16 v[90:91], v69 offset:0\n"
    "ds_read_b64_tr_b16 v[92:93], v69 offset:4096\n"
    "ds_read_b64_tr_b16 v[94:95], v68 offset:8192\n"
    "ds_read_b64_tr_b16 v[96:97], v68 offset:12288\n"
    "ds_read_b64_tr_b16 v[98:99], v69 offset:8192\n"
    "ds_read_b64_tr_b16 v[100:101], v69 offset:12288\n"
    "ds_read_b64_tr_b16 v[102:103], v68 offset:16384\n"
    "ds_read_b64_tr_b16 v[104:105], v68 offset:20480\n"
    "ds_read_b64_tr_b16 v[106:107], v69 offset:16384\n"
    "ds_read_b64_tr_b16 v[108:109], v69 offset:20480\n"
    "s_waitcnt lgkmcnt(10)\n"
    "v_mfma_f32_16x16x32_bf16 v[134:137], v[70:73], v[86:89], 0\n"
    "s_waitcnt lgkmcnt(8)\n"
    "v_mfma_f32_16x16x32_bf16 v[138:141], v[70:73], v[90:93], 0\n"
    "s_waitcnt lgkmcnt(6)\n"
    "v_mfma_f32_16x16x32_bf16 v[134:137], v[74:77], v[94:97], v[134:137]\n"
    "s_waitcnt lgkmcnt(4)\n"
    "v_mfma_f32_16x16x32_bf16 v[138:141], v[74:77], v[98:101], v[138:141]\n"
    "s_waitcnt lgkmcnt(2)\n"
    "v_mfma_f32_16x16x32_bf16 v[134:137], v[78:81], v[102:105], v[134:137]\n"
    "s_waitcnt lgkmcnt(0)\n"
    "v_mfma_f32_16x16x32_bf16 v[138:141], v[78:81], v[106:109], v[138:141]\n"
    ".Lw20:\n"
    "ds_read_b32 v11, v14 offset:5404\n"
    "ds_read_b64_tr_b16 v[110:111], v68 offset:24576\n"
    "ds_read_b64_tr_b16 v[112:113], v68 offset:28672\n"
    "ds_read_b64_tr_b16 v[114:115], v69 offset:24576\n"
    "ds_read_b64_tr_b16 v[116:117], v69 offset:28672\n"
    "s_waitcnt lgkmcnt(4)\n"
    "v_cmp_ne_u32 vcc, 1, v11\n"
    "s_cbranch_vccnz .Lw20\n"
    "s_waitcnt lgkmcnt(2)\n"
    "v_mfma_f32_16x16x32_bf16 v[134:137], v[82:85], v[110:113], v[134:137]\n"
    "s_waitcnt lgkmcnt(0)\n"
    "v_mfma_f32_16x16x32_bf16 v[138:141], v[82:85], v[114:117], v[138:141]\n"
    "s_nop 7\n"
    "v_cvt_pk_bf16_f32 v5, v134, v138\n"
    "v_min_u32 v5, -2, v5\n"
    "global_store_dword v50, v5, s[14:15] sc1\n"
    "s_waitcnt vmcnt(1)\n"
    ".Lh_chk:\n"
    "v_xor_b32 v158, v59, v158\n"
    "v_xor_b32 v159, v60, v159\n"
    "v_or_b32 v5, v158, v159\n"
    "v_cmp_ne_u32_sdwa vcc, v5, v14 src0_sel:BYTE_0 src1_sel:DWORD\n"
    "s_cbranch_vccnz .Lh_retry\n"
    ".Lh_ok:\n"
    "v_permlane32_swap_b32 v158, v159\n"
    "v_add_f32 v158, v158, v159\n"
    "v_mov_b32 v5, v158\n"
    "s_nop 1\n"
    "v_permlane16_swap_b32 v158, v5\n"
    "v_add_f32 v158, v158, v5\n"
    "s_nop 1\n"
    "v_add_f32_dpp v158, v158, v158 row_ror:8 row_mask:0xf bank_mask:0xf bound_ctrl:1\n"
    "v_lshlrev_b32 v63, 16, v63\n"
    "v_cvt_pk_bf16_f32 v158, 0, v158\n"
    "v_add_f32 v158, v158, v63\n"
    "v_cvt_pk_bf16_f32 v158, 0, v158\n"
    "v_mul_f32 v5, s62, -|v158|\n"
    "v_exp_f32 v5, v5\n"
    "v_cmp_lt_f32 vcc, 0, v158\n"
    "v_add_f32 v6, 2.0, v5\n"
    "v_mul_f32 v6, v6, v5\n"
    "v_fma_f32 v7, v5, 2.0, 1.0\n"
    "v_cndmask_b32 v6, v6, v7, vcc\n"
    "v_mul_f32 v5, v5, v5\n"
    "v_cndmask_b32 v5, 1.0, v5, vcc\n"
    "v_fma_f32 v5, v5, 2.0, v6\n"
    "v_rcp_f32 v5, v5\n"
    "v_cmp_gt_u32 vcc, 56, v62\n"
    "v_mul_f32 v5, v5, v6\n"
    "v_mul_f32 v5, v5, v158\n"
    "v_cvt_pk_bf16_f32 v5, v5, v5\n"
    "v_cndmask_b32 v5, 0, v5, vcc\n"
    "s_mov_b64 exec, s[54:55]\n"
    "ds_write_b16 v55, v5 offset:5120\n"
    "s_mov_b64 exec, -1\n"
    ".Lp21:\n"
    "s_memrealtime s[30:31]\n"
    "s_waitcnt lgkmcnt(0)\n"
    "s_sub_u32 s49, s30, s28\n"
    "s_cmpk_lt_u32 s49, 444\n"
    "s_cbranch_scc1 .Lp21\n"
    "global_load_dwordx4 v[150:153], v52, s[14:15] sc1\n"
    "global_load_dwordx4 v[154:157], v52, s[14:15] offset:1024 sc1\n"
    ".Lw22:\n"
    "ds_read_b32 v11, v14 offset:5408\n"
    "s_waitcnt lgkmcnt(0)\n"
    "v_cmp_ne_u32 vcc, 1, v11\n"
    "s_cbranch_vccnz .Lw22\n"
    "ds_read_b64_tr_b16 v[70:71], v35 offset:40960\n"
    "ds_read_b64_tr_b16 v[72:73], v35 offset:41472\n"
    "ds_read_b64_tr_b16 v[74:75], v35 offset:45056\n"
    "ds_read_b64_tr_b16 v[76:77], v35 offset:45568\n"
    "ds_read_b64_tr_b16 v[78:79], v35 offset:49152\n"
    "ds_read_b64_tr_b16 v[80:81], v35 offset:49664\n"
    "ds_read_b64_tr_b16 v[82:83], v35 offset:53248\n"
    "ds_read_b64_tr_b16 v[84:85], v35 offset:53760\n"
    "s_lshl_b32 s50, s39, 9\n"
    "v_add_u32 v182, s50, v56\n"
    "v_mov_b32 v175, s42\n"
    "s_waitcnt vmcnt(1)\n"
    ".Lo_chk:\n"
    "ds_write_b128 v53, v[150:153] offset:8192\n"
    "s_waitcnt vmcnt(0)\n"
    "ds_write_b128 v54, v[154:157] offset:9216\n"
    "v_max3_u32 v5, v150, v151, v152\n"
    "v_max3_u32 v6, v154, v155, v156\n"
    "v_max3_u32 v5, v5, v153, v157\n"
    "v_max_u32 v5, v5, v6\n"
    "v_cmp_eq_u32 vcc, -1, v5\n"
    "s_cbranch_vccnz .Lo_retry\n"
    ".Lo_ok:\n"
    "s_mov_b64 exec, -1\n"
    "s_waitcnt lgkmcnt(0)\n"
    "s_barrier\n"
    "ds_read_b128 v[86:89], v16 offset:8192\n"
    "ds_read_b128 v[90:93], v16 offset:12288\n"
    "ds_read_b128 v[94:97], v17 offset:8192\n"
    "ds_read_b128 v[98:101], v17 offset:12288\n"
    "ds_read_b128 v[118:121], v38 offset:5888\n"
    "ds_read_b128 v[122:125], v38 offset:5952\n"
    "ds_read_b128 v[102:105], v18 offset:8192\n"
    "ds_read_b128 v[106:109], v18 offset:12288\n"
    "ds_read_b128 v[110:113], v19 offset:8192\n"
    "ds_read_b128 v[114:117], v19 offset:12288\n"
    "s_waitcnt lgkmcnt(9)\n"
    "v_mfma_f32_16x16x32_bf16 v[134:137], v[86:89], v[70:73], 0\n"
    "ds_read_b128 v[126:129], v38 offset:5120\n"
    "ds_read_b64_tr_b16 v[162:163], v35 offset:32768\n"
    "ds_read_b64_tr_b16 v[164:165], v35 offset:33280\n"
    "ds_read_b128 v[130:133], v38 offset:5184\n"
    "ds_read_b64_tr_b16 v[166:167], v35 offset:36864\n"
    "ds_read_b64_tr_b16 v[168:169], v35 offset:37376\n"
    "s_waitcnt lgkmcnt(14)\n"
    "v_mfma_f32_16x16x32_bf16 v[138:141], v[90:93], v[70:73], 0\n"
    "s_waitcnt lgkmcnt(13)\n"
    "v_mfma_f32_16x16x32_bf16 v[134:137], v[94:97], v[74:77], v[134:137]\n"
    "s_waitcnt lgkmcnt(12)\n"
    "v_mfma_f32_16x16x32_bf16 v[138:141], v[98:101], v[74:77], v[138:141]\n"
    "s_waitcnt lgkmcnt(9)\n"
    "v_mfma_f32_16x16x32_bf16 v[134:137], v[102:105], v[78:81], v[134:137]\n"
    "s_waitcnt lgkmcnt(8)\n"
    "v_mfma_f32_16x16x32_bf16 v[138:141], v[106:109], v[78:81], v[138:141]\n"
    "s_waitcnt lgkmcnt(7)\n"
    "v_mfma_f32_16x16x32_bf16 v[134:137], v[110:113], v[82:85], v[134:137]\n"
    "s_waitcnt lgkmcnt(6)\n"
    "v_mfma_f32_16x16x32_bf16 v[138:141], v[114:117], v[82:85], v[138:141]\n"
    "s_waitcnt lgkmcnt(3)\n"
    "v_mfma_f32_16x16x32_bf16 v[142:145], v[126:129], v[162:165], 0\n"
    "s_waitcnt lgkmcnt(0)\n"
    "v_mfma_f32_16x16x32_bf16 v[142:145], v[130:133], v[166:169], v[142:145]\n"
    "s_nop 1\n"
    "v_pk_mul_f32 v[176:177], v[118:119], v[134:135]\n"
    "v_pk_fma_f32 v[176:177], v[120:121], v[136:137], v[176:177]\n"
    "v_pk_fma_f32 v[176:177], v[122:123], v[138:139], v[176:177]\n"
    "v_pk_fma_f32 v[176:177], v[124:125], v[140:141], v[176:177]\n"
    "v_add_f32 v7, v176, v177\n"
    "v_add_f32 v6, v176, v177\n"
    "v_mul_f32 v178, v147, v142\n"
    "v_cvt_f64_f32 v[180:181], v178\n"
    "v_permlane32_swap_b32 v7, v6\n"
    "v_add_f32 v5, v7, v6\n"
    "v_add_f32 v6, v7, v6\n"
    "v_add_f64 v[180:181], v[180:181], s[56:57]\n"
    "s_nop 0\n"
    "v_permlane16_swap_b32 v5, v6\n"
    "s_mov_b64 exec, 0xffff\n"
    "v_add_f32 v12, v5, v6\n"
    "v_cvt_f64_f32 v[170:171], v12\n"
    "v_fma_f64 v[170:171], v[170:171], v[148:149], v[180:181]\n"
    "v_subrev_u32 v5, 0x40f00000, v171\n"
    "v_subrev_u32 v171, 0x3cf00000, v171\n"
    "v_cmp_le_u32 vcc, 0x8000, v5\n"
    "s_cbranch_vccnz .Lrange\n"
    ".Lrange_done:\n"
    "global_atomic_add_x2 v[172:173], v13, v[170:171], s[14:15] sc0 sc1\n"
    "v_fma_f32 v174, v146, v12, v178\n"
    "global_store_dwordx2 v182, v[174:175], s[14:15] sc1\n"
    "s_mov_b32 s49, 0xfffff\n"
    "v_mov_b32 v7, 0x40f00000\n"
    "s_mov_b32 s50, 0x35800000\n"
    "v_mov_b32 v180, v170\n"
    "v_bfi_b32 v181, s49, v171, v7\n"
    "v_add_f64 v[180:181], v[180:181], -s[58:59]\n"
    "s_waitcnt vmcnt(1)\n"
    "v_cmp_le_u32 vcc, 0x7c000000, v173\n"
    "v_or_b32 v6, v173, v171\n"
    "v_bfi_b32 v173, s49, v173, v7\n"
    "s_and_b64 exec, exec, vcc\n"
    "s_cbranch_execz .Lend\n"
    "v_add_f64 v[172:173], v[172:173], v[180:181]\n"
    "v_cvt_f32_f64 v5, v[172:173]\n"
    "v_cvt_pk_bf16_f32 v7, v5, v5\n"
    "global_store_short v57, v7, s[24:25]\n"
    "v_and_b32 v6, 0x03f00000, v6\n"
    "v_cmp_ne_u32 s[60:61], 0, v6\n"
    "v_cmp_gt_f32 vcc, s50, |v5|\n"
    "s_or_b64 vcc, vcc, s[60:61]\n"
    "s_cbranch_vccnz .Lslow\n"
    ".Lfin:\n"
    "v_cmp_eq_u32 vcc, 0, v57\n"
    "s_and_b64 exec, exec, vcc\n"
    "s_cbranch_execz .Lend\n"
    "global_store_dword v57, v175, s[14:15]\n"
    ".Lend:\n"
    "s_endpgm\n"
    ".Lm_retry:\n"
    "s_mov_b64 exec, vcc\n"
    "global_load_dwordx4 v[28:31], v34, s[14:15] sc1\n"
    "s_sub_u32 s48, s48, 1\n"
    "s_waitcnt vmcnt(0)\n"
    "s_cbranch_scc0 .Lm_chk\n"
    "s_mov_b32 s48, 0\n"
    "s_branch .Lm_ok\n"
    ".Lq_retry:\n"
    "s_mov_b64 exec, vcc\n"
    "global_load_dwordx4 v[150:153], v41, s[14:15] sc1\n"
    "global_load_dwordx4 v[154:157], v41, s[14:15] offset:2048 sc1\n"
    "s_sub_u32 s48, s48, 1\n"
    "s_waitcnt vmcnt(0)\n"
    "s_cbranch_scc0 .Lq_chk\n"
    "s_mov_b32 s48, 0\n"
    "s_branch .Lq_ok\n"
    ".Lh_retry:\n"
    "global_load_dwordx2 v[158:159], v42, s[14:15] sc1\n"
    "s_sub_u32 s48, s48, 1\n"
    "s_waitcnt vmcnt(0)\n"
    "s_cbranch_scc0 .Lh_chk\n"
    "s_mov_b32 s48, 0\n"
    "s_branch .Lh_ok\n"
    ".Lo_retry:\n"
    "s_mov_b64 exec, vcc\n"
    "global_load_dwordx4 v[150:153], v52, s[14:15] sc1\n"
    "global_load_dwordx4 v[154:157], v52, s[14:15] offset:1024 sc1\n"
    "s_sub_u32 s48, s48, 1\n"
    "s_waitcnt vmcnt(0)\n"
    "s_cbranch_scc0 .Lo_chk\n"
    "s_mov_b32 s48, 0\n"
    "s_branch .Lo_ok\n"
    ".Lrange:\n"
    "v_mov_b32 v5, 0x4104000\n"
    "s_nop 0\n"
    "v_cndmask_b32 v170, v170, v14, vcc\n"
    "v_cndmask_b32 v171, v171, v5, vcc\n"
    "s_branch .Lrange_done\n"
    ".Lslow:\n"
    "s_mov_b64 s[60:61], exec\n"
    "s_mov_b64 s[52:53], vcc\n"
    "s_mov_b64 exec, -1\n"
    "v_lshl_add_u32 v56, v3, 12, v56\n"
    ".Lslow1:\n"
    "global_load_dwordx2 v[70:71], v56, s[14:15] offset:0 sc1\n"
    "global_load_dwordx2 v[72:73], v56, s[14:15] offset:512 sc1\n"
    "global_load_dwordx2 v[74:75], v56, s[14:15] offset:1024 sc1\n"
    "global_load_dwordx2 v[76:77], v56, s[14:15] offset:1536 sc1\n"
    "global_load_dwordx2 v[78:79], v56, s[14:15] offset:2048 sc1\n"
    "global_load_dwordx2 v[80:81], v56, s[14:15] offset:2560 sc1\n"
    "global_load_dwordx2 v[82:83], v56, s[14:15] offset:3072 sc1\n"
    "global_load_dwordx2 v[84:85], v56, s[14:15] offset:3584 sc1\n"
    "s_waitcnt vmcnt(0)\n"
    "v_xor_b32 v71, s42, v71\n"
    "v_xor_b32 v73, s42, v73\n"
    "v_xor_b32 v75, s42, v75\n"
    "v_xor_b32 v77, s42, v77\n"
    "v_xor_b32 v79, s42, v79\n"
    "v_xor_b32 v81, s42, v81\n"
    "v_xor_b32 v83, s42, v83\n"
    "v_xor_b32 v85, s42, v85\n"
    "v_or_b32 v5, v71, v73\n"
    "v_or3_b32 v5, v5, v75, v77\n"
    "v_or3_b32 v5, v5, v79, v81\n"
    "v_or3_b32 v5, v5, v83, v85\n"
    "v_cmp_ne_u32 vcc, 0, v5\n"
    "s_cbranch_vccz .Lslow2\n"
    "s_sub_u32 s48, s48, 1\n"
    "s_cbranch_scc0 .Lslow1\n"
    ".Lslow2:\n"
    "v_add_f32 v5, v70, v72\n"
    "v_add_f32 v5, v5, v74\n"
    "v_add_f32 v5, v5, v76\n"
    "v_add_f32 v5, v5, v78\n"
    "v_add_f32 v5, v5, v80\n"
    "v_add_f32 v5, v5, v82\n"
    "v_add_f32 v5, v5, v84\n"
    "v_mov_b32 v6, v5\n"
    "s_nop 1\n"
    "v_permlane16_swap_b32 v5, v6\n"
    "v_add_f32 v5, v5, v6\n"
    "v_mov_b32 v6, v5\n"
    "s_nop 1\n"
    "v_permlane32_swap_b32 v5, v6\n"
    "v_add_f32 v5, v5, v6\n"
    "s_mov_b64 exec, s[52:53]\n"
    "v_cvt_pk_bf16_f32 v7, v5, v5\n"
    "global_store_short v57, v7, s[24:25]\n"
    "s_mov_b64 exec, s[60:61]\n"
    "s_branch .Lfin\n"
    :
    : "{s[0:1]}"(__builtin_amdgcn_kernarg_segment_ptr()), "{s2}"(blockIdx.x), "{v0}"(threadIdx.x),
      "s"((__attribute__((address_space(3))) char*)lds)
    : "v1", "v2", "v3", "v4", "v5", "v6", "v7", "v8", "v9", "v10", "v11", "v12", "v13", "v14", "v15", "v16",
      "v17", "v18", "v19", "v20", "v21", "v22", "v23", "v24", "v25", "v26", "v27", "v28", "v29", "v30", "v31", "v32",
      "v33", "v34", "v35", "v36", "v37", "v38", "v39", "v40", "v41", "v42", "v43", "v44", "v45", "v46", "v47", "v48",
      "v49", "v50", "v51", "v52", "v53", "v54", "v55", "v56", "v57", "v58", "v59", "v60", "v61", "v62", "v63", "v64",
      "v65", "v66", "v67", "v68", "v69", "v70", "v71", "v72", "v73", "v74", "v75", "v76", "v77", "v78", "v79", "v80",
      "v81", "v82", "v83", "v84", "v85", "v86", "v87", "v88", "v89", "v90", "v91", "v92", "v93", "v94", "v95", "v96",
      "v97", "v98", "v99", "v100", "v101", "v102", "v103", "v104", "v105", "v106", "v107", "v108", "v109", "v110", "v111", "v112",
      "v113", "v114", "v115", "v116", "v117", "v118", "v119", "v120", "v121", "v122", "v123", "v124", "v125", "v126", "v127", "v128",
      "v129", "v130", "v131", "v132", "v133", "v134", "v135", "v136", "v137", "v138", "v139", "v140", "v141", "v142", "v143", "v144",
      "v145", "v146", "v147", "v148", "v149", "v150", "v151", "v152", "v153", "v154", "v155", "v156", "v157", "v158", "v159", "v160",
      "v161", "v162", "v163", "v164", "v165", "v166", "v167", "v168", "v169", "v170", "v171", "v172", "v173", "v174", "v175", "v176",
      "v177", "v178", "v179", "v180", "v181", "v182", "s3", "s4", "s5", "s6", "s7", "s8", "s9", "s10", "s11", "s12",
      "s13", "s14", "s15", "s16", "s17", "s18", "s19", "s20", "s21", "s22", "s23", "s24", "s25", "s26", "s27", "s28",
      "s29", "s30", "s31", "s34", "s35", "s36", "s37", "s38", "s39", "s40", "s41", "s42", "s43", "s44", "s45", "s46",
      "s47", "s48", "s49", "s50", "s51", "s52", "s53", "s54", "s55", "s56", "s57", "s58", "s59", "s60", "s61", "s62",
      "s63", "s64", "s65", "vcc", "scc", "memory");
}

extern "C" void kernel_launch(void* const* d_in, const int* in_sizes, int n_in, void* d_out, int out_size, void* d_ws,
                              size_t ws_size, hipStream_t stream) {
  const size_t win = (size_t)(65536 - 4096) * 128 * 2;
  gm_kernel<<<256, 384, 0, stream>>>((const char*)d_in[3], (const char*)d_in[0], (const char*)d_in[9], (const char*)d_in[6],
                                     (const char*)d_in[1] + win, (char*)d_ws, (const char*)d_in[2] + win, (const char*)d_in[4],
                                     (const char*)d_in[8], (const char*)d_in[7], (char*)d_out);
}
```
